```python
import torch

def _softmax(x, axis=-1):
    return torch.softmax(x, dim=axis)
def _gelu(x):
    return torch.nn.functional.gelu(x, approximate="tanh")
def _silu(x):
    return torch.nn.functional.silu(x)
def _sigmoid(x):
    return torch.sigmoid(x)
def _layernorm(x, axis=-1, eps=1e-5):
    m = x.mean(dim=axis, keepdim=True)
    v = x.var(dim=axis, keepdim=True, unbiased=False)
    return (x - m) / torch.sqrt(v + eps)
def _scatter_add(tgt, idx, src, axis=0):
    return tgt.clone().index_add_(axis, idx, src)
def _segment_sum(src, seg, n):
    out = torch.zeros((n,) + src.shape[1:], dtype=src.dtype)
    return out.index_add_(0, seg, src)
def _pad(x, pw):
    flat = tuple(v for pair in reversed(pw) for v in pair)
    return torch.nn.functional.pad(x, flat)

kernel_name = 'parallel_sum__mha_block_cached__swiglu_ffn__decode'

def setup_inputs():

    _w = (lambda a: (lambda t: t.to(torch.bfloat16) if t.is_floating_point() else t)(torch.as_tensor(a)))
    def _A_setup():
        B, Sq, D = 4, 1, 640
        H, Sk, Dh = 32, 4096, 128
        return {
            "x": _w((lambda *s: torch.randn(*s, dtype=torch.bfloat16) * 0.05)(B, Sq, D)),
            "k_cache": _w((lambda *s: torch.randn(*s, dtype=torch.bfloat16) * 3.2)(B, H, Sk, Dh)),
            "v_cache": _w((lambda *s: torch.randn(*s, dtype=torch.bfloat16) * 1.6)(B, H, Sk, Dh)),
            "wq": _w((lambda *s: torch.randn(*s, dtype=torch.bfloat16) * 3.2)(D, H * Dh) * (D ** -0.5)),
            "wo": _w((lambda *s: torch.randn(*s, dtype=torch.bfloat16) * 1.6)(H * Dh, D) * ((H * Dh) ** -0.5)),
        }
    _A_in = _A_setup()

    def _B_setup():
        B, S, D, F = 4, 1, 640, 1728
        return {
            "x": _w((lambda *s: torch.randn(*s, dtype=torch.bfloat16) * 0.05)(B, S, D)),
            "w_gate": _w((lambda *s: torch.randn(*s, dtype=torch.bfloat16) * 0.05)(D, F)),
            "w_up": _w((lambda *s: torch.randn(*s, dtype=torch.bfloat16) * 0.05)(D, F)),
            "w_down": _w((lambda *s: torch.randn(*s, dtype=torch.bfloat16) * 0.05)(F, D)),
        }
    _B_in = _B_setup()

    return {
        "A_x": _A_in["x"],
        "A_k_cache": _A_in["k_cache"],
        "A_v_cache": _A_in["v_cache"],
        "A_wq": _A_in["wq"],
        "A_wo": _A_in["wo"],
        "B_x": _B_in["x"],
        "B_w_gate": _B_in["w_gate"],
        "B_w_up": _B_in["w_up"],
        "B_w_down": _B_in["w_down"],
    }

def reference(A_x, A_k_cache, A_v_cache, A_wq, A_wo, B_x, B_w_gate, B_w_up, B_w_down):
    def _A_ref(x, k_cache, v_cache, wq, wo):
        B, Sq, D = x.shape
        H, Dh = 32, 128
        q = (lambda x, *d: x.permute(*d))((x @ wq).reshape(B, Sq, H, Dh), 0, 2, 1, 3)
        scores = torch.einsum("bhqd,bhkd->bhqk", q, k_cache) * (Dh ** -0.5)
        p = _softmax(scores, axis=-1)
        o = torch.einsum("bhqk,bhkd->bhqd", p, v_cache)
        return (lambda x, *d: x.permute(*d))(o, 0, 2, 1, 3).reshape(B, Sq, H * Dh) @ wo

    def _B_ref(x, w_gate, w_up, w_down):
        g = _silu(x @ w_gate)
        u = x @ w_up
        return (g * u) @ w_down

    x = A_x
    xn = _layernorm(x, axis=-1)
    return x + _A_ref(xn, A_k_cache, A_v_cache, A_wq, A_wo) + _B_ref(xn, B_w_gate, B_w_up, B_w_down)
```

```cpp
#include <hip/hip_runtime.h>
#include <hip/hip_bf16.h>
#include <stdint.h>

typedef unsigned u32x4 __attribute__((ext_vector_type(4)));
typedef float f32x4 __attribute__((ext_vector_type(4)));
typedef __bf16 bf16x2 __attribute__((ext_vector_type(2)));

constexpr int B = 4, D = 640, H = 32, SK = 4096, DH = 128, F = 1728;
constexpr int NSPLIT = 4;
#define MIMIC 0

__device__ __forceinline__ float bf2f(unsigned short u) { return __builtin_bit_cast(float, (unsigned)u << 16); }
__device__ __forceinline__ float blo(unsigned u) { return __builtin_bit_cast(float, u << 16); }
__device__ __forceinline__ float bhi(unsigned u) { return __builtin_bit_cast(float, u & 0xffff0000u); }
__device__ __forceinline__ unsigned short f2bf(float f) { __bf16 b = (__bf16)f; return __builtin_bit_cast(unsigned short, b); }
__device__ __forceinline__ float rbf(float f) { return bf2f(f2bf(f)); }
__device__ __forceinline__ float dot2(unsigned k, unsigned q, float acc) {
  return __builtin_amdgcn_fdot2_f32_bf16(__builtin_bit_cast(bf16x2, k), __builtin_bit_cast(bf16x2, q), acc, false);
}
__device__ __forceinline__ float wave_sum(float v) { for (int o = 32; o >= 1; o >>= 1) v += __shfl_xor(v, o); return v; }
__device__ __forceinline__ float wave_max(float v) { for (int o = 32; o >= 1; o >>= 1) v = fmaxf(v, __shfl_xor(v, o)); return v; }

__global__ void __launch_bounds__(256) k_pre(const unsigned short* x, const unsigned short* wq, const unsigned short* wg, const unsigned short* wu,
                                             float* q, float* hbuf) {
  __shared__ float xn[B][D];
  __shared__ float red[2][32][B][64];
  const int tid = threadIdx.x, wave = tid >> 6, lane = tid & 63;
  {
    float v[10]; float s = 0.f;
    for (int i = 0; i < 10; ++i) { v[i] = bf2f(x[wave * D + lane + 64 * i]); s += v[i]; }
    s = wave_sum(s); float m = s / D;
    float s2 = 0.f;
    for (int i = 0; i < 10; ++i) { float d = v[i] - m; s2 += d * d; }
    s2 = wave_sum(s2) / D;
    float r = 1.f / sqrtf(s2 + 1e-5f);
    for (int i = 0; i < 10; ++i) xn[wave][lane + 64 * i] = (v[i] - m) * r;
  }
  __syncthreads();
  const int c8 = tid & 7, rg = tid >> 3;
  const bool isq = blockIdx.x < 64;
  const int nmat = isq ? 1 : 2;
  for (int mt = 0; mt < nmat; ++mt) {
    const unsigned short* w = isq ? wq : (mt == 0 ? wg : wu);
    const int ld = isq ? H * DH : F;
    const int col0 = (isq ? blockIdx.x : blockIdx.x - 64) * 64 + c8 * 8;
    float acc[B][8] = {};
    for (int r = rg; r < D; r += 32) {
      u32x4 wv = *(const u32x4*)(w + (size_t)r * ld + col0);
      float wf[8] = {blo(wv[0]), bhi(wv[0]), blo(wv[1]), bhi(wv[1]), blo(wv[2]), bhi(wv[2]), blo(wv[3]), bhi(wv[3])};
      for (int b = 0; b < B; ++b) { float xv = xn[b][r]; for (int k = 0; k < 8; ++k) acc[b][k] += xv * wf[k]; }
    }
    for (int b = 0; b < B; ++b) for (int k = 0; k < 8; ++k) red[mt][rg][b][c8 * 8 + k] = acc[b][k];
  }
  __syncthreads();
  { const int b = tid >> 6, c = tid & 63;
    float s0 = 0.f, s1 = 0.f;
    for (int g = 0; g < 32; ++g) { s0 += red[0][g][b][c]; if (!isq) s1 += red[1][g][b][c]; }
    if (isq) q[b * (H * DH) + blockIdx.x * 64 + c] = s0;
    else {
      float g = s0 / (1.f + __expf(-s0)); hbuf[b * F + (blockIdx.x - 64) * 64 + c] = g * s1;
    }
  }
}

__global__ void __launch_bounds__(256) k_attn(const float* q, const unsigned short* kc, const unsigned short* vc, float* pml, float* po) {
  constexpr int KEYS = SK / NSPLIT;
  __shared__ float sc[KEYS];
  __shared__ float redm[4], redl[4];
  __shared__ float ored[16][128];
  const int s = blockIdx.x, h = blockIdx.y, b = blockIdx.z;
  const int tid = threadIdx.x, wave = tid >> 6, lane = tid & 63, r = lane >> 4, c = lane & 15;
  const size_t base = ((size_t)(b * H + h) * SK + (size_t)s * KEYS) * DH;
  unsigned qp[4];
  for (int k = 0; k < 4; ++k) {
    float lo = q[b * (H * DH) + h * DH + c * 8 + 2 * k], hi = q[b * (H * DH) + h * DH + c * 8 + 2 * k + 1];
    qp[k] = (unsigned)f2bf(lo) | ((unsigned)f2bf(hi) << 16);
  }
  for (int it = 0; it < KEYS / 4 / 16; ++it) {
    u32x4 kv[4];
    for (int t = 0; t < 4; ++t) { int key = wave * (KEYS / 4) + it * 16 + t * 4 + r; kv[t] = __builtin_nontemporal_load((const u32x4*)(kc + base + (size_t)key * DH + c * 8)); }
    for (int t = 0; t < 4; ++t) {
      float d = 0.f; for (int k = 0; k < 4; ++k) d = dot2(kv[t][k], qp[k], d);
      d += __shfl_xor(d, 1); d += __shfl_xor(d, 2); d += __shfl_xor(d, 4); d += __shfl_xor(d, 8);
      d = d * 0.08838834764831845f;
      if (c == 0) sc[wave * (KEYS / 4) + it * 16 + t * 4 + r] = d;
    }
  }
  __syncthreads();
  float m = -INFINITY;
  for (int i = tid; i < KEYS; i += 256) m = fmaxf(m, sc[i]);
  m = wave_max(m); if (lane == 0) redm[wave] = m;
  __syncthreads();
  m = fmaxf(fmaxf(redm[0], redm[1]), fmaxf(redm[2], redm[3]));
  float l = 0.f;
  for (int i = tid; i < KEYS; i += 256) { float p = __expf(sc[i] - m); sc[i] = p; l += p; }
  l = wave_sum(l); if (lane == 0) redl[wave] = l;
  __syncthreads();
  l = redl[0] + redl[1] + redl[2] + redl[3];
  float acc[8] = {};
  for (int it = 0; it < KEYS / 4 / 16; ++it) {
    u32x4 vv[4];
    for (int t = 0; t < 4; ++t) { int key = wave * (KEYS / 4) + it * 16 + t * 4 + r; vv[t] = __builtin_nontemporal_load((const u32x4*)(vc + base + (size_t)key * DH + c * 8)); }
    for (int t = 0; t < 4; ++t) {
      float p = sc[wave * (KEYS / 4) + it * 16 + t * 4 + r];
      for (int k = 0; k < 4; ++k) { acc[2 * k] += p * blo(vv[t][k]); acc[2 * k + 1] += p * bhi(vv[t][k]); }
    }
  }
  for (int k = 0; k < 8; ++k) ored[wave * 4 + r][c * 8 + k] = acc[k];
  __syncthreads();
  if (tid < 128) {
    float o = 0.f; for (int g = 0; g < 16; ++g) o += ored[g][tid];
    po[((size_t)(b * H + h) * NSPLIT + s) * DH + tid] = o;
    if (tid == 0) { pml[((b * H + h) * NSPLIT + s) * 2] = m; pml[((b * H + h) * NSPLIT + s) * 2 + 1] = l; }
  }
}

__global__ void __launch_bounds__(256) k_post(const unsigned short* x, const float* pml, const float* po, const float* hbuf, const unsigned short* wo, const unsigned short* wd,
                                              unsigned short* out) {
  __shared__ float o[H * DH];
  __shared__ float hh[F];
  __shared__ float red[2][32][64];
  const int tid = threadIdx.x, b = blockIdx.y, cg = blockIdx.x;
  for (int i = tid; i < H * DH; i += 256) {
    int h = i >> 7, d = i & 127;
    const float* ml = pml + (b * H + h) * NSPLIT * 2;
    float M = -INFINITY; for (int s = 0; s < NSPLIT; ++s) M = fmaxf(M, ml[2 * s]);
    float L = 0.f, a = 0.f;
    for (int s = 0; s < NSPLIT; ++s) { float w = __expf(ml[2 * s] - M); L += w * ml[2 * s + 1]; a += w * po[((size_t)(b * H + h) * NSPLIT + s) * DH + d]; }
    o[i] = a / L;
  }
  for (int i = tid; i < F; i += 256) hh[i] = hbuf[b * F + i];
  __syncthreads();
  const int c8 = tid & 7, rg = tid >> 3;
  float acc[8] = {}, acc2[8] = {};
  for (int r = rg; r < H * DH; r += 32) {
    u32x4 wv = *(const u32x4*)(wo + (size_t)r * D + cg * 64 + c8 * 8); float xv = o[r];
    for (int k = 0; k < 4; ++k) { acc[2 * k] += xv * blo(wv[k]); acc[2 * k + 1] += xv * bhi(wv[k]); }
  }
  for (int r = rg; r < F; r += 32) {
    u32x4 wv = *(const u32x4*)(wd + (size_t)r * D + cg * 64 + c8 * 8); float xv = hh[r];
    for (int k = 0; k < 4; ++k) { acc2[2 * k] += xv * blo(wv[k]); acc2[2 * k + 1] += xv * bhi(wv[k]); }
  }
  for (int k = 0; k < 8; ++k) { red[0][rg][c8 * 8 + k] = acc[k]; red[1][rg][c8 * 8 + k] = acc2[k]; }
  __syncthreads();
  if (tid < 64) {
    float a = 0.f, f = 0.f; for (int g = 0; g < 32; ++g) { a += red[0][g][tid]; f += red[1][g][tid]; }
    float xv = bf2f(x[b * D + cg * 64 + tid]);
    out[b * D + cg * 64 + tid] = f2bf(xv + a + f);
  }
}

extern "C" void kernel_launch(void* const* d_in, const int* in_sizes, int n_in, void* d_out, int out_size, void* d_ws, size_t ws_size, hipStream_t stream) {
  const unsigned short* x = (const unsigned short*)d_in[0];
  const unsigned short* kc = (const unsigned short*)d_in[1];
  const unsigned short* vc = (const unsigned short*)d_in[2];
  const unsigned short* wq = (const unsigned short*)d_in[3];
  const unsigned short* wo = (const unsigned short*)d_in[4];
  const unsigned short* wg = (const unsigned short*)d_in[6];
  const unsigned short* wu = (const unsigned short*)d_in[7];
  const unsigned short* wd = (const unsigned short*)d_in[8];
  float* q = (float*)d_ws;
  float* hbuf = q + B * H * DH;
  float* pml = hbuf + 8192;
  float* po = pml + 4096;
  k_pre<<<64 + 27, 256, 0, stream>>>(x, wq, wg, wu, q, hbuf);
  k_attn<<<dim3(NSPLIT, H, B), 256, 0, stream>>>(q, kc, vc, pml, po);
  k_post<<<dim3(10, B), 256, 0, stream>>>(x, pml, po, hbuf, wo, wd, (unsigned short*)d_out);
}
```

```cpp
#include <hip/hip_runtime.h>
#include <hip/hip_bf16.h>
#include <stdint.h>

typedef unsigned u32x4 __attribute__((ext_vector_type(4)));
typedef unsigned u32x2 __attribute__((ext_vector_type(2)));
typedef float f32x4 __attribute__((ext_vector_type(4)));
typedef float f32x2 __attribute__((ext_vector_type(2)));
typedef __bf16 bf16x2 __attribute__((ext_vector_type(2)));

#define W_AUX 0
constexpr int KV_AUX = 2;
constexpr int SC1 = 16;
constexpr int D = 640, H = 32, F = 1728;
constexpr unsigned WS_EPOCH = 0, WS_ML = 4096, WS_Y = 65536, WS_BYTES = WS_Y + 4 * 64 * 5120;
constexpr unsigned SPIN_CAP = 1u << 18;

#define LD128(r, vo, so, aux) __builtin_amdgcn_raw_buffer_load_b128(r, vo, so, aux)
#define LD64(r, vo, so, aux) __builtin_amdgcn_raw_buffer_load_b64(r, vo, so, aux)
#define LD32(r, vo, so, aux) __builtin_amdgcn_raw_buffer_load_b32(r, vo, so, aux)
#define SB() __builtin_amdgcn_sched_barrier(0)
#define RING 1
#define RSB() __builtin_amdgcn_sched_barrier(0)
#define CB() asm volatile("" ::: "memory")
#define BARRIER() asm volatile("s_waitcnt lgkmcnt(0)\n\ts_barrier" ::: "memory")

__device__ __forceinline__ float u2f(unsigned u) { return __builtin_bit_cast(float, u); }
__device__ __forceinline__ unsigned f2u(float f) { return __builtin_bit_cast(unsigned, f); }
__device__ __forceinline__ float blo(unsigned u) { return u2f(u << 16); }
__device__ __forceinline__ float bhi(unsigned u) { return u2f(u & 0xffff0000u); }
__device__ __forceinline__ unsigned pk(float lo, float hi) {
  f32x2 v = {lo, hi};
  return __builtin_bit_cast(unsigned, __builtin_convertvector(v, bf16x2));
}
__device__ __forceinline__ float rbf(float f) { return blo(pk(f, f)); }
__device__ __forceinline__ float dot2(unsigned a, unsigned b, float acc) {
  return __builtin_amdgcn_fdot2_f32_bf16(__builtin_bit_cast(bf16x2, a), __builtin_bit_cast(bf16x2, b), acc, false);
}
template <int CTRL> __device__ __forceinline__ float dpp(float x) {
  return u2f((unsigned)__builtin_amdgcn_mov_dpp((int)f2u(x), CTRL, 0xf, 0xf, true));
}
constexpr int XOR1 = 0xB1, XOR2 = 0x4E, HMIR = 0x141, ROR8 = 0x128, ROR4 = 0x124, SHL4 = 0x104;
__device__ __forceinline__ float swap32_add(float a, float b) {
  auto t = __builtin_amdgcn_permlane32_swap(f2u(a), f2u(b), false, false);
  return u2f(t[0]) + u2f(t[1]);
}
__device__ __forceinline__ float swap16_add(float a, float b) {
  auto t = __builtin_amdgcn_permlane16_swap(f2u(a), f2u(b), false, false);
  return u2f(t[0]) + u2f(t[1]);
}
__device__ __forceinline__ float xrow_sum(float x) { x = swap16_add(x, x); return swap32_add(x, x); }
__device__ __forceinline__ float xrow_max(float x) {
  auto s = __builtin_amdgcn_permlane16_swap(f2u(x), f2u(x), false, false);
  x = fmaxf(u2f(s[0]), u2f(s[1]));
  auto t = __builtin_amdgcn_permlane32_swap(f2u(x), f2u(x), false, false);
  return fmaxf(u2f(t[0]), u2f(t[1]));
}
__device__ __forceinline__ float row_sum(float v) { v += dpp<XOR1>(v); v += dpp<XOR2>(v); v += dpp<HMIR>(v); v += dpp<ROR8>(v); return v; }
__device__ __forceinline__ float row_max(float v) {
  v = fmaxf(v, dpp<XOR1>(v)); v = fmaxf(v, dpp<XOR2>(v)); v = fmaxf(v, dpp<HMIR>(v)); v = fmaxf(v, dpp<ROR8>(v)); return v;
}
__device__ __forceinline__ float wave_sum(float v) { return xrow_sum(row_sum(v)); }
__device__ __forceinline__ float wave_max(float v) { return xrow_max(row_max(v)); }
__device__ __forceinline__ unsigned plo(unsigned a, unsigned b) { return __builtin_amdgcn_perm(b, a, 0x05040100u); }
__device__ __forceinline__ unsigned phi(unsigned a, unsigned b) { return __builtin_amdgcn_perm(b, a, 0x07060302u); }
__device__ __forceinline__ float exp_(float x) { return __builtin_amdgcn_exp2f(x * 1.4426950408889634f); }

#define STAMP(i) do { } while (0)

struct Smem {
  unsigned xn[8][320];
  float qpart[8][128];
  float fpart[8][64];
  unsigned qv[8][64];
  float sc[8][256];
  float opart[8][128];
  float ypart[8][640];
  float red[8][128];
};

__global__ void __launch_bounds__(512, 2)
fused_decode(const unsigned short* __restrict__ x, const unsigned short* __restrict__ kc, const unsigned short* __restrict__ vc,
             const unsigned short* __restrict__ wq, const unsigned short* __restrict__ wo, const unsigned short* __restrict__ wg,
             const unsigned short* __restrict__ wu, const unsigned short* __restrict__ wd, unsigned short* __restrict__ out,
             unsigned char* ws) {
  __shared__ __attribute__((aligned(16))) Smem sm;
  const int tid = threadIdx.x, lane = tid & 63;
  const int w = __builtin_amdgcn_readfirstlane(tid >> 6);
  const int bid = blockIdx.x, x8 = bid & 7, rr_ = bid >> 3, half = rr_ & 1, b = (rr_ >> 1) & 3, hhi = rr_ >> 3, h = hhi * 8 + x8;
  const int r = lane >> 4, c = lane & 15;
  STAMP(0);
  asm volatile("" :: "s"(x), "s"(kc), "s"(vc), "s"(wq), "s"(wo), "s"(wg), "s"(wu), "s"(wd), "s"(out), "s"(ws));

  const unsigned region = (unsigned)((b * H + h) * 2 + half);
  auto rX = __builtin_amdgcn_make_buffer_rsrc((void*)x, 0, 4 * D * 2, 0x00020000);
  auto rK = __builtin_amdgcn_make_buffer_rsrc((void*)(kc + (size_t)region * (2048 * 128)), 0, 512 * 1024, 0x00020000);
  auto rV = __builtin_amdgcn_make_buffer_rsrc((void*)(vc + (size_t)region * (2048 * 128)), 0, 512 * 1024, 0x00020000);
  auto rWq = __builtin_amdgcn_make_buffer_rsrc((void*)wq, 0, D * 4096 * 2, 0x00020000);
  auto rWo = __builtin_amdgcn_make_buffer_rsrc((void*)wo, 0, D * 4096 * 2, 0x00020000);
  auto rWg = __builtin_amdgcn_make_buffer_rsrc((void*)wg, 0, D * F * 2, 0x00020000);
  auto rWu = __builtin_amdgcn_make_buffer_rsrc((void*)wu, 0, D * F * 2, 0x00020000);
  auto rWs = __builtin_amdgcn_make_buffer_rsrc((void*)ws, 0, WS_BYTES, 0x00020000);
  const int slot = hhi * 2 + half, nch = slot < 3 ? 4 : 3, fcol = x8 * 216 + 8 * (slot * 3 + (slot < 3 ? slot : 3));
  auto rWd = __builtin_amdgcn_make_buffer_rsrc((void*)wd, 0, (w < 2 * nch) ? F * D * 2 : 0, 0x00020000);
  const int rj = (h - b * 5) & 31;
  const bool is_red = (half == 0) && (rj < 5);
  const int gp = rj * 64 + lane;
  const int ocol = gp < 128 ? 8 * (gp >> 1) + 2 * (gp & 1) : (gp < 256 ? 8 * ((gp - 128) >> 1) + 4 + 2 * (gp & 1) : 512 + 2 * (gp - 256));

  const u32x4 xa = LD128(rX, lane * 16, b * 1280, 0);
  const unsigned xb = LD32(rX, 1024 + lane * 4, b * 1280, 0);
  const unsigned xo = LD32(rX, is_red ? ocol * 2 : 0, b * 1280, 0);
  const u32x2 ep = LD64(rWs, 0, WS_EPOCH + b * 256, SC1);
  SB();
  u32x4 wqv[20];
  {
    const unsigned vo = r * (20 * 8192) + c * 16, so = w * (80 * 8192) + h * 256;
#pragma unroll
    for (int i = 0; i < 20; ++i) wqv[i] = LD128(rWq, vo, so + i * 8192, W_AUX);
  }
  SB();
  u32x4 gv[5], uv[5];
  {
    const unsigned vo = (lane & 3) < nch ? (lane >> 2) * (5 * 3456) + (lane & 3) * 16 : 0x80000000u, so = w * (80 * 3456) + fcol * 2;
#pragma unroll
    for (int i = 0; i < 5; ++i) { gv[i] = LD128(rWg, vo, so + i * 3456, W_AUX); uv[i] = LD128(rWu, vo, so + i * 3456, W_AUX); }
  }
  SB();
  u32x4 buf[4];
  const unsigned kvo = lane * 16;
#pragma unroll
  for (int j = 0; j < 4; ++j) buf[j] = LD128(rK, kvo + j * 1024, w * 4096, KV_AUX);
  SB();
  u32x4 da[4]; unsigned db[4];
#pragma unroll
  for (int i = 0; i < 4; ++i) { const unsigned so = (fcol + 4 * w + i) * 1280; da[i] = LD128(rWd, lane * 16, so, W_AUX); db[i] = LD32(rWd, 1024 + lane * 4, so, W_AUX); }
  SB();
  u32x4 oa[16]; unsigned ob[16];
#pragma unroll
  for (int i = 0; i < 8; ++i) { const unsigned so = (h * 128 + 16 * w + i) * 1280; oa[i] = LD128(rWo, lane * 16, so, W_AUX); ob[i] = LD32(rWo, 1024 + lane * 4, so, W_AUX); }
  SB();

  {
    float v[10] = {blo(xa[0]), bhi(xa[0]), blo(xa[1]), bhi(xa[1]), blo(xa[2]), bhi(xa[2]), blo(xa[3]), bhi(xa[3]), blo(xb), bhi(xb)};
    float s = 0.f;
#pragma unroll
    for (int i = 0; i < 10; ++i) s += v[i];
    const float mean = wave_sum(s) * (1.f / D);
    float s2 = 0.f;
#pragma unroll
    for (int i = 0; i < 10; ++i) { const float d = v[i] - mean; s2 += d * d; }
    const float var = wave_sum(s2) * (1.f / D);
    const float mb = rbf(mean), den = rbf(__builtin_sqrtf(rbf(rbf(var) + 1e-5f))), rd = 1.f / den;
#pragma unroll
    for (int i = 0; i < 10; ++i) v[i] = rbf(v[i] - mb) * rd;
    u32x4 o4 = {pk(v[0], v[1]), pk(v[2], v[3]), pk(v[4], v[5]), pk(v[6], v[7])};
    *(u32x4*)&sm.xn[w][lane * 4] = o4;
    sm.xn[w][256 + lane] = pk(v[8], v[9]);
  }
  CB();
  {
    unsigned xp[10];
    const unsigned* xs = &sm.xn[w][40 * w + 10 * r];
#pragma unroll
    for (int i = 0; i < 5; ++i) { const u32x2 t = *(const u32x2*)(xs + 2 * i); xp[2 * i] = t[0]; xp[2 * i + 1] = t[1]; }
    float a[8] = {0.f, 0.f, 0.f, 0.f, 0.f, 0.f, 0.f, 0.f};
#pragma unroll
    for (int i = 0; i < 10; ++i)
#pragma unroll
      for (int k = 0; k < 4; ++k) {
        a[2 * k] = dot2(plo(wqv[2 * i][k], wqv[2 * i + 1][k]), xp[i], a[2 * k]);
        a[2 * k + 1] = dot2(phi(wqv[2 * i][k], wqv[2 * i + 1][k]), xp[i], a[2 * k + 1]);
      }
    float e[4];
#pragma unroll
    for (int k = 0; k < 4; ++k) e[k] = swap32_add(a[k], a[k + 4]);
    f32x2 g = {swap16_add(e[0], e[2]), swap16_add(e[1], e[3])};
    *(f32x2*)&sm.qpart[w][8 * c + 2 * r] = g;
  }
  SB();
#pragma unroll
  for (int i = 8; i < 16; ++i) { const unsigned so = (h * 128 + 16 * w + i) * 1280; oa[i] = LD128(rWo, lane * 16, so, W_AUX); ob[i] = LD32(rWo, 1024 + lane * 4, so, W_AUX); }
  SB();
  {
    const unsigned short* xs = (const unsigned short*)&sm.xn[w][0] + 80 * w + 5 * (lane >> 2);
    float a[16];
#pragma unroll
    for (int k = 0; k < 16; ++k) a[k] = 0.f;
#pragma unroll
    for (int i = 0; i < 5; ++i) {
      const unsigned xl = xs[i], xh = xl << 16;
#pragma unroll
      for (int k = 0; k < 4; ++k) {
        a[2 * k] = dot2(gv[i][k], xl, a[2 * k]); a[2 * k + 1] = dot2(gv[i][k], xh, a[2 * k + 1]);
        a[8 + 2 * k] = dot2(uv[i][k], xl, a[8 + 2 * k]); a[9 + 2 * k] = dot2(uv[i][k], xh, a[9 + 2 * k]);
      }
    }
    float e[8];
#pragma unroll
    for (int k = 0; k < 8; ++k) e[k] = swap32_add(a[k], a[k + 8]);
    f32x4 f;
#pragma unroll
    for (int k = 0; k < 4; ++k) { float t = swap16_add(e[k], e[k + 4]); t += dpp<ROR4>(t); t += dpp<ROR8>(t); f[k] = t; }
    if ((lane & 12) == 0) *(f32x4*)&sm.fpart[w][(lane >> 5) * 32 + (lane & 3) * 8 + ((lane >> 4) & 1) * 4] = f;
  }
  STAMP(1);
  BARRIER();
  u32x4 qp;
  {
    f32x2 s = *(const f32x2*)&sm.qpart[0][2 * lane];
#pragma unroll
    for (int i = 1; i < 8; ++i) s += *(const f32x2*)&sm.qpart[i][2 * lane];
    sm.qv[w][lane] = pk(s[0], s[1]);
    CB();
    qp = *(const u32x4*)&sm.qv[w][4 * c];
  }
  const float fsum = sm.fpart[lane >> 3][((lane >> 2) & 1) * 32 + 4 * w + (lane & 3)];
  STAMP(2);

  const bool b3 = lane & 8, b2 = lane & 4;
  float* scw = &sm.sc[w][r * 4 + ((lane >> 3) & 1) + ((lane >> 1) & 2)];
  auto kblock = [&](int it, auto rsrc, unsigned so) __attribute__((always_inline)) {
    float a[4];
#pragma unroll
    for (int j = 0; j < 4; ++j) {
      float t = dot2(buf[j][0], qp[0], 0.f); t = dot2(buf[j][1], qp[1], t); t = dot2(buf[j][2], qp[2], t); a[j] = dot2(buf[j][3], qp[3], t);
      RSB();
      buf[j] = LD128(rsrc, kvo + j * 1024, so, KV_AUX);
      RSB();
    }
    const float u0 = (b3 ? a[1] : a[0]) + dpp<ROR8>(b3 ? a[0] : a[1]);
    const float u1 = (b3 ? a[3] : a[2]) + dpp<ROR8>(b3 ? a[2] : a[3]);
    float v = (b2 ? u1 : u0) + dpp<HMIR>(b2 ? u0 : u1);
    v += dpp<XOR1>(v); v += dpp<XOR2>(v);
    v = rbf(rbf(v) * 0.08838834764831845f);
    if ((lane & 3) == 0) scw[it * 16] = v;
  };
  kblock(0, rK, (8 + w) * 4096);
  SB();
  float y[10];
  unsigned wp[8][10];
  {
    float v = fsum + dpp<ROR8>(fsum);
    v = rbf(xrow_sum(v));
    const float gs = rbf(v / (1.f + exp_(-v)));
    const float hh = gs * dpp<SHL4>(v);
    const unsigned hp = pk(hh, dpp<XOR1>(hh));
    const unsigned h01 = __builtin_amdgcn_readlane(hp, 0), h23 = __builtin_amdgcn_readlane(hp, 2);
#pragma unroll
    for (int k = 0; k < 4; ++k) {
      y[2 * k] = dot2(plo(da[2][k], da[3][k]), h23, dot2(plo(da[0][k], da[1][k]), h01, 0.f));
      y[2 * k + 1] = dot2(phi(da[2][k], da[3][k]), h23, dot2(phi(da[0][k], da[1][k]), h01, 0.f));
    }
    y[8] = dot2(plo(db[2], db[3]), h23, dot2(plo(db[0], db[1]), h01, 0.f));
    y[9] = dot2(phi(db[2], db[3]), h23, dot2(phi(db[0], db[1]), h01, 0.f));
#pragma unroll
    for (int i = 0; i < 8; ++i) {
#pragma unroll
      for (int k = 0; k < 4; ++k) { wp[i][2 * k] = plo(oa[2 * i][k], oa[2 * i + 1][k]); wp[i][2 * k + 1] = phi(oa[2 * i][k], oa[2 * i + 1][k]); }
      wp[i][8] = plo(ob[2 * i], ob[2 * i + 1]); wp[i][9] = phi(ob[2 * i], ob[2 * i + 1]);
#pragma unroll
      for (int k = 0; k < 10; ++k) asm volatile("" : "+v"(wp[i][k]));
    }
#pragma unroll
    for (int k = 0; k < 10; ++k) asm volatile("" : "+v"(y[k]));
  }
  SB();
  STAMP(3);
#pragma unroll 1
  for (int it = 1; it < 15; ++it) kblock(it, rK, ((it + 1) * 8 + w) * 4096);
  kblock(15, rV, w * 4096);
  SB();
  STAMP(4);

  const unsigned tagA = ep[0] + 1u, tagB = ep[1] + 1u;
  float mw;
  CB();
  {
    const f32x4 s4 = *(const f32x4*)&sm.sc[w][lane * 4];
    mw = wave_max(fmaxf(fmaxf(s4[0], s4[1]), fmaxf(s4[2], s4[3])));
    const float p0 = exp_(s4[0] - mw), p1 = exp_(s4[1] - mw), p2 = exp_(s4[2] - mw), p3 = exp_(s4[3] - mw);
    const unsigned q01 = pk(p0, p1), q23 = pk(p2, p3);
    u32x4 pw = {q01 & 0xffffu, q01 >> 16, q23 & 0xffffu, q23 >> 16};
    *(u32x4*)&sm.sc[w][lane * 4] = pw;
    const float lw = wave_sum((p0 + p1) + (p2 + p3));
    if (lane == 0) {
      u32x4 g = {f2u(mw), tagA, f2u(lw), tagB};
      __builtin_amdgcn_raw_buffer_store_b128(g, rWs, 0, WS_ML + (b * H + h) * 256 + (half * 8 + w) * 16, SC1);
    }
  }
  CB();
  float acc[8] = {0.f, 0.f, 0.f, 0.f, 0.f, 0.f, 0.f, 0.f};
  const unsigned* pr = (const unsigned*)&sm.sc[w][r * 4];
  auto vblock = [&](int it, bool more) __attribute__((always_inline)) {
    const u32x4 pv = *(const u32x4*)(pr + it * 16);
#pragma unroll
    for (int j = 0; j < 4; ++j) {
      const unsigned pl = pv[j], ph = pl << 16;
#pragma unroll
      for (int k = 0; k < 4; ++k) { acc[2 * k] = dot2(buf[j][k], pl, acc[2 * k]); acc[2 * k + 1] = dot2(buf[j][k], ph, acc[2 * k + 1]); }
      RSB();
      if (more) buf[j] = LD128(rV, kvo + j * 1024, ((it + 1) * 8 + w) * 4096, KV_AUX);
      RSB();
    }
#pragma unroll
    for (int k = 0; k < 8; ++k) asm volatile("" : "+v"(acc[k]));
  };
#pragma unroll 1
  for (int it = 0; it < 13; ++it) vblock(it, true);
  SB();
  u32x4 ml = LD128(rWs, c * 16, WS_ML + (b * H + h) * 256, SC1);
  SB();
  vblock(13, true);
  SB();
  vblock(14, true);
  SB();
  float fw;
  {
    if (__builtin_expect(!__all(ml[1] == tagA && ml[3] == tagB), 0)) {
      unsigned spins = 0;
      do {
        __builtin_amdgcn_s_sleep(2);
        CB();
        ml = LD128(rWs, c * 16, WS_ML + (b * H + h) * 256, SC1);
      } while (!__all(ml[1] == tagA && ml[3] == tagB) && ++spins < SPIN_CAP);
    }
    const float mi = u2f(ml[0]), M = row_max(mi);
    const float L = row_sum(u2f(ml[2]) * exp_(mi - M));
    fw = exp_(mw - M) / L;
  }
  SB();
  vblock(15, false);
  STAMP(5);
  {
    float e[4];
#pragma unroll
    for (int k = 0; k < 4; ++k) e[k] = swap32_add(acc[k], acc[k + 4]);
    f32x2 g = {swap16_add(e[0], e[2]) * fw, swap16_add(e[1], e[3]) * fw};
    *(f32x2*)&sm.opart[w][8 * c + 2 * r] = g;
  }
  BARRIER();
  {
    float v = sm.opart[2 * r][16 * w + c] + sm.opart[2 * r + 1][16 * w + c];
    v = xrow_sum(v);
    const unsigned op = pk(v, dpp<XOR1>(v));
#pragma unroll
    for (int i = 0; i < 8; ++i) {
      const unsigned oi = __builtin_amdgcn_readlane(op, 2 * i);
#pragma unroll
      for (int k = 0; k < 10; ++k) y[k] = dot2(wp[i][k], oi, y[k]);
    }
    f32x4 y0 = {y[0], y[1], y[2], y[3]}, y1 = {y[4], y[5], y[6], y[7]};
    f32x2 y2 = {y[8], y[9]};
    *(f32x4*)&sm.ypart[w][lane * 4] = y0;
    *(f32x4*)&sm.ypart[w][256 + lane * 4] = y1;
    *(f32x2*)&sm.ypart[w][512 + lane * 2] = y2;
  }
  BARRIER();
  if (w < 5) {
    f32x2 s = *(const f32x2*)&sm.ypart[0][2 * tid];
#pragma unroll
    for (int i = 1; i < 8; ++i) s += *(const f32x2*)&sm.ypart[i][2 * tid];
    u32x4 g = {f2u(s[0]), tagA, f2u(s[1]), tagB};
    __builtin_amdgcn_raw_buffer_store_b128(g, rWs, tid * 16, WS_Y + (b * 64 + h * 2 + half) * 5120, SC1);
  }
  STAMP(6);
  if (!is_red) return;
  {
    u32x4 g[8];
    const unsigned so = WS_Y + (b * 64 + w * 8) * 5120 + rj * 1024;
#pragma unroll
    for (int i = 0; i < 8; ++i) g[i] = LD128(rWs, lane * 16, so + i * 5120, SC1);
    unsigned spins = 0;
    for (;;) {
      bool all = true;
      CB();
#pragma unroll
      for (int i = 0; i < 8; ++i) {
        if (!__all(g[i][1] == tagA && g[i][3] == tagB)) { all = false; g[i] = LD128(rWs, lane * 16, so + i * 5120, SC1); }
      }
      if (all || ++spins > SPIN_CAP) break;
      __builtin_amdgcn_s_sleep(1);
    }
    f32x2 s = {0.f, 0.f};
#pragma unroll
    for (int i = 0; i < 8; ++i) { s[0] += u2f(g[i][0]); s[1] += u2f(g[i][2]); }
    *(f32x2*)&sm.red[w][2 * lane] = s;
  }
  BARRIER();
  if (w == 0) {
    f32x2 s = *(const f32x2*)&sm.red[0][2 * lane];
#pragma unroll
    for (int i = 1; i < 8; ++i) s += *(const f32x2*)&sm.red[i][2 * lane];
    *(unsigned*)(out + b * D + ocol) = pk(blo(xo) + s[0], bhi(xo) + s[1]);
    if (rj == 0 && lane == 0) {
      u32x2 e = {tagA, tagB};
      __builtin_amdgcn_raw_buffer_store_b64(e, rWs, 0, WS_EPOCH + b * 256, SC1);
    }
  }
  STAMP(7);
}

extern "C" void kernel_launch(void* const* d_in, const int* in_sizes, int n_in, void* d_out, int out_size, void* d_ws, size_t ws_size,
                              hipStream_t stream) {
  fused_decode<<<256, 512, 0, stream>>>((const unsigned short*)d_in[0], (const unsigned short*)d_in[1], (const unsigned short*)d_in[2],
                                        (const unsigned short*)d_in[3], (const unsigned short*)d_in[4], (const unsigned short*)d_in[6],
                                        (const unsigned short*)d_in[7], (const unsigned short*)d_in[8], (unsigned short*)d_out,
                                        (unsigned char*)d_ws);
}
```

```cpp
#include <hip/hip_runtime.h>
#include <hip/hip_bf16.h>
#include <stdint.h>

typedef unsigned u32x4 __attribute__((ext_vector_type(4)));
typedef unsigned u32x2 __attribute__((ext_vector_type(2)));
typedef float f32x4 __attribute__((ext_vector_type(4)));
typedef float f32x2 __attribute__((ext_vector_type(2)));
typedef __bf16 bf16x2 __attribute__((ext_vector_type(2)));

#define W_AUX 0
#define K_AUX 2
#define V_AUX 2
constexpr int SC1 = 16;
constexpr int D = 640, H = 32, F = 1728;
constexpr unsigned WS_EPOCH = 0, WS_ML = 4096, WS_Y = 65536, WS_BYTES = WS_Y + 4 * 64 * 5120;
constexpr unsigned SPIN_CAP = 1u << 18;

#define LD128(r, vo, so, aux) __builtin_amdgcn_raw_buffer_load_b128(r, vo, so, aux)
#define LD64(r, vo, so, aux) __builtin_amdgcn_raw_buffer_load_b64(r, vo, so, aux)
#define LD32(r, vo, so, aux) __builtin_amdgcn_raw_buffer_load_b32(r, vo, so, aux)
#define SB() __builtin_amdgcn_sched_barrier(0)
#define RING 1
#define RSB() __builtin_amdgcn_sched_barrier(0)
#define CB() asm volatile("" ::: "memory")
#define BARRIER() asm volatile("s_waitcnt lgkmcnt(0)\n\ts_barrier" ::: "memory")

__device__ __forceinline__ float u2f(unsigned u) { return __builtin_bit_cast(float, u); }
__device__ __forceinline__ unsigned f2u(float f) { return __builtin_bit_cast(unsigned, f); }
__device__ __forceinline__ float blo(unsigned u) { return u2f(u << 16); }
__device__ __forceinline__ float bhi(unsigned u) { return u2f(u & 0xffff0000u); }
__device__ __forceinline__ unsigned pk(float lo, float hi) {
  f32x2 v = {lo, hi};
  return __builtin_bit_cast(unsigned, __builtin_convertvector(v, bf16x2));
}
__device__ __forceinline__ float rbf(float f) { return blo(pk(f, f)); }
__device__ __forceinline__ float dot2(unsigned a, unsigned b, float acc) {
  return __builtin_amdgcn_fdot2_f32_bf16(__builtin_bit_cast(bf16x2, a), __builtin_bit_cast(bf16x2, b), acc, false);
}
template <int CTRL> __device__ __forceinline__ float dpp(float x) {
  return u2f((unsigned)__builtin_amdgcn_mov_dpp((int)f2u(x), CTRL, 0xf, 0xf, true));
}
constexpr int XOR1 = 0xB1, XOR2 = 0x4E, HMIR = 0x141, ROR8 = 0x128, ROR4 = 0x124, SHL4 = 0x104;
__device__ __forceinline__ float swap32_add(float a, float b) {
  auto t = __builtin_amdgcn_permlane32_swap(f2u(a), f2u(b), false, false);
  return u2f(t[0]) + u2f(t[1]);
}
__device__ __forceinline__ float swap16_add(float a, float b) {
  auto t = __builtin_amdgcn_permlane16_swap(f2u(a), f2u(b), false, false);
  return u2f(t[0]) + u2f(t[1]);
}
__device__ __forceinline__ float xrow_sum(float x) { x = swap16_add(x, x); return swap32_add(x, x); }
__device__ __forceinline__ float xrow_max(float x) {
  auto s = __builtin_amdgcn_permlane16_swap(f2u(x), f2u(x), false, false);
  x = fmaxf(u2f(s[0]), u2f(s[1]));
  auto t = __builtin_amdgcn_permlane32_swap(f2u(x), f2u(x), false, false);
  return fmaxf(u2f(t[0]), u2f(t[1]));
}
__device__ __forceinline__ float row_sum(float v) { v += dpp<XOR1>(v); v += dpp<XOR2>(v); v += dpp<HMIR>(v); v += dpp<ROR8>(v); return v; }
__device__ __forceinline__ float row_max(float v) {
  v = fmaxf(v, dpp<XOR1>(v)); v = fmaxf(v, dpp<XOR2>(v)); v = fmaxf(v, dpp<HMIR>(v)); v = fmaxf(v, dpp<ROR8>(v)); return v;
}
__device__ __forceinline__ float wave_sum(float v) { return xrow_sum(row_sum(v)); }
__device__ __forceinline__ float wave_max(float v) { return xrow_max(row_max(v)); }
__device__ __forceinline__ unsigned plo(unsigned a, unsigned b) { return __builtin_amdgcn_perm(b, a, 0x05040100u); }
__device__ __forceinline__ unsigned phi(unsigned a, unsigned b) { return __builtin_amdgcn_perm(b, a, 0x07060302u); }
__device__ __forceinline__ float exp_(float x) { return __builtin_amdgcn_exp2f(x * 1.4426950408889634f); }

#define STAMP(i) do { } while (0)

struct Smem {
  unsigned xn[8][320];
  float qpart[8][128];
  float fpart[8][64];
  unsigned qv[8][64];
  float sc[8][256];
  float opart[8][128];
  float ypart[8][640];
  float red[8][128];
};

__global__ void __launch_bounds__(512, 2)
fused_decode(const unsigned short* __restrict__ x, const unsigned short* __restrict__ kc, const unsigned short* __restrict__ vc,
             const unsigned short* __restrict__ wq, const unsigned short* __restrict__ wo, const unsigned short* __restrict__ wg,
             const unsigned short* __restrict__ wu, const unsigned short* __restrict__ wd, unsigned short* __restrict__ out,
             unsigned char* ws) {
  __shared__ __attribute__((aligned(16))) Smem sm;
  const int tid = threadIdx.x, lane = tid & 63;
  const int w = __builtin_amdgcn_readfirstlane(tid >> 6);
  const int bid = blockIdx.x, x8 = bid & 7, rr_ = bid >> 3, half = rr_ & 1, b = (rr_ >> 1) & 3, hhi = rr_ >> 3, h = hhi * 8 + x8;
  const int r = lane >> 4, c = lane & 15;
  STAMP(0);
  asm volatile("" :: "s"(x), "s"(kc), "s"(vc), "s"(wq), "s"(wo), "s"(wg), "s"(wu), "s"(wd), "s"(out), "s"(ws));

  const unsigned region = (unsigned)((b * H + h) * 2 + half);
  auto rX = __builtin_amdgcn_make_buffer_rsrc((void*)x, 0, 4 * D * 2, 0x00020000);
  auto rK = __builtin_amdgcn_make_buffer_rsrc((void*)(kc + (size_t)region * (2048 * 128)), 0, 512 * 1024, 0x00020000);
  auto rV = __builtin_amdgcn_make_buffer_rsrc((void*)(vc + (size_t)region * (2048 * 128)), 0, 512 * 1024, 0x00020000);
  auto rWq = __builtin_amdgcn_make_buffer_rsrc((void*)wq, 0, D * 4096 * 2, 0x00020000);
  auto rWo = __builtin_amdgcn_make_buffer_rsrc((void*)wo, 0, D * 4096 * 2, 0x00020000);
  auto rWg = __builtin_amdgcn_make_buffer_rsrc((void*)wg, 0, D * F * 2, 0x00020000);
  auto rWu = __builtin_amdgcn_make_buffer_rsrc((void*)wu, 0, D * F * 2, 0x00020000);
  auto rWs = __builtin_amdgcn_make_buffer_rsrc((void*)ws, 0, WS_BYTES, 0x00020000);
  const int slot = hhi * 2 + half, nch = slot < 3 ? 4 : 3, fcol = x8 * 216 + 8 * (slot * 3 + (slot < 3 ? slot : 3));
  auto rWd = __builtin_amdgcn_make_buffer_rsrc((void*)wd, 0, (w < 2 * nch) ? F * D * 2 : 0, 0x00020000);
  const int rj = (h - b * 5) & 31;
  const bool is_red = (half == 0) && (rj < 5);
  const int gp = rj * 64 + lane;
  const int ocol = gp < 128 ? 8 * (gp >> 1) + 2 * (gp & 1) : (gp < 256 ? 8 * ((gp - 128) >> 1) + 4 + 2 * (gp & 1) : 512 + 2 * (gp - 256));

  const u32x4 xa = LD128(rX, lane * 16, b * 1280, 0);
  const unsigned xb = LD32(rX, 1024 + lane * 4, b * 1280, 0);
  const unsigned xo = LD32(rX, is_red ? ocol * 2 : 0, b * 1280, 0);
  const u32x2 ep = LD64(rWs, 0, WS_EPOCH + b * 256, SC1);
  SB();
  u32x4 wqv[20];
  {
    const unsigned vo = r * (20 * 8192) + c * 16, so = w * (80 * 8192) + h * 256;
#pragma unroll
    for (int i = 0; i < 20; ++i) wqv[i] = LD128(rWq, vo, so + i * 8192, W_AUX);
  }
  SB();
  u32x4 gv[5], uv[5];
  {
    const unsigned vo = (lane & 3) < nch ? (lane >> 2) * (5 * 3456) + (lane & 3) * 16 : 0x80000000u, so = w * (80 * 3456) + fcol * 2;
#pragma unroll
    for (int i = 0; i < 5; ++i) { gv[i] = LD128(rWg, vo, so + i * 3456, W_AUX); uv[i] = LD128(rWu, vo, so + i * 3456, W_AUX); }
  }
  SB();
  u32x4 buf[4];
  const unsigned kvo = lane * 16;
#pragma unroll
  for (int j = 0; j < 4; ++j) buf[j] = LD128(rK, kvo + j * 1024, w * 4096, K_AUX);
  SB();
  u32x4 da[4]; unsigned db[4];
#pragma unroll
  for (int i = 0; i < 4; ++i) { const unsigned so = (fcol + 4 * w + i) * 1280; da[i] = LD128(rWd, lane * 16, so, W_AUX); db[i] = LD32(rWd, 1024 + lane * 4, so, W_AUX); }
  SB();
  u32x4 oa[16]; unsigned ob[16];
#pragma unroll
  for (int i = 0; i < 8; ++i) { const unsigned so = (h * 128 + 16 * w + i) * 1280; oa[i] = LD128(rWo, lane * 16, so, W_AUX); ob[i] = LD32(rWo, 1024 + lane * 4, so, W_AUX); }
  SB();

  {
    float v[10] = {blo(xa[0]), bhi(xa[0]), blo(xa[1]), bhi(xa[1]), blo(xa[2]), bhi(xa[2]), blo(xa[3]), bhi(xa[3]), blo(xb), bhi(xb)};
    float s = 0.f;
#pragma unroll
    for (int i = 0; i < 10; ++i) s += v[i];
    const float mean = wave_sum(s) * (1.f / D);
    float s2 = 0.f;
#pragma unroll
    for (int i = 0; i < 10; ++i) { const float d = v[i] - mean; s2 += d * d; }
    const float var = wave_sum(s2) * (1.f / D);
    const float mb = rbf(mean), den = rbf(__builtin_sqrtf(rbf(rbf(var) + 1e-5f))), rd = 1.f / den;
#pragma unroll
    for (int i = 0; i < 10; ++i) v[i] = rbf(v[i] - mb) * rd;
    u32x4 o4 = {pk(v[0], v[1]), pk(v[2], v[3]), pk(v[4], v[5]), pk(v[6], v[7])};
    *(u32x4*)&sm.xn[w][lane * 4] = o4;
    sm.xn[w][256 + lane] = pk(v[8], v[9]);
  }
  CB();
  {
    unsigned xp[10];
    const unsigned* xs = &sm.xn[w][40 * w + 10 * r];
#pragma unroll
    for (int i = 0; i < 5; ++i) { const u32x2 t = *(const u32x2*)(xs + 2 * i); xp[2 * i] = t[0]; xp[2 * i + 1] = t[1]; }
    float a[8] = {0.f, 0.f, 0.f, 0.f, 0.f, 0.f, 0.f, 0.f};
#pragma unroll
    for (int i = 0; i < 10; ++i)
#pragma unroll
      for (int k = 0; k < 4; ++k) {
        a[2 * k] = dot2(plo(wqv[2 * i][k], wqv[2 * i + 1][k]), xp[i], a[2 * k]);
        a[2 * k + 1] = dot2(phi(wqv[2 * i][k], wqv[2 * i + 1][k]), xp[i], a[2 * k + 1]);
      }
    float e[4];
#pragma unroll
    for (int k = 0; k < 4; ++k) e[k] = swap32_add(a[k], a[k + 4]);
    f32x2 g = {swap16_add(e[0], e[2]), swap16_add(e[1], e[3])};
    *(f32x2*)&sm.qpart[w][8 * c + 2 * r] = g;
  }
  SB();
#pragma unroll
  for (int i = 8; i < 16; ++i) { const unsigned so = (h * 128 + 16 * w + i) * 1280; oa[i] = LD128(rWo, lane * 16, so, W_AUX); ob[i] = LD32(rWo, 1024 + lane * 4, so, W_AUX); }
  SB();
  {
    const unsigned short* xs = (const unsigned short*)&sm.xn[w][0] + 80 * w + 5 * (lane >> 2);
    float a[16];
#pragma unroll
    for (int k = 0; k < 16; ++k) a[k] = 0.f;
#pragma unroll
    for (int i = 0; i < 5; ++i) {
      const unsigned xl = xs[i], xh = xl << 16;
#pragma unroll
      for (int k = 0; k < 4; ++k) {
        a[2 * k] = dot2(gv[i][k], xl, a[2 * k]); a[2 * k + 1] = dot2(gv[i][k], xh, a[2 * k + 1]);
        a[8 + 2 * k] = dot2(uv[i][k], xl, a[8 + 2 * k]); a[9 + 2 * k] = dot2(uv[i][k], xh, a[9 + 2 * k]);
      }
    }
    float e[8];
#pragma unroll
    for (int k = 0; k < 8; ++k) e[k] = swap32_add(a[k], a[k + 8]);
    f32x4 f;
#pragma unroll
    for (int k = 0; k < 4; ++k) { float t = swap16_add(e[k], e[k + 4]); t += dpp<ROR4>(t); t += dpp<ROR8>(t); f[k] = t; }
    if ((lane & 12) == 0) *(f32x4*)&sm.fpart[w][(lane >> 5) * 32 + (lane & 3) * 8 + ((lane >> 4) & 1) * 4] = f;
  }
  STAMP(1);
  BARRIER();
  u32x4 qp;
  {
    f32x2 s = *(const f32x2*)&sm.qpart[0][2 * lane];
#pragma unroll
    for (int i = 1; i < 8; ++i) s += *(const f32x2*)&sm.qpart[i][2 * lane];
    sm.qv[w][lane] = pk(s[0], s[1]);
    CB();
    qp = *(const u32x4*)&sm.qv[w][4 * c];
  }
  const float fsum = sm.fpart[lane >> 3][((lane >> 2) & 1) * 32 + 4 * w + (lane & 3)];
  STAMP(2);

  const bool b3 = lane & 8, b2 = lane & 4;
  float* scw = &sm.sc[w][r * 4 + ((lane >> 3) & 1) + ((lane >> 1) & 2)];
  auto kblock = [&](int it, auto rsrc, unsigned so, bool isv) __attribute__((always_inline)) {
    float a[4];
#pragma unroll
    for (int j = 0; j < 4; ++j) {
      float t = dot2(buf[j][0], qp[0], 0.f); t = dot2(buf[j][1], qp[1], t); t = dot2(buf[j][2], qp[2], t); a[j] = dot2(buf[j][3], qp[3], t);
      RSB();
      buf[j] = isv ? LD128(rsrc, kvo + j * 1024, so, V_AUX) : LD128(rsrc, kvo + j * 1024, so, K_AUX);
      RSB();
    }
    const float u0 = (b3 ? a[1] : a[0]) + dpp<ROR8>(b3 ? a[0] : a[1]);
    const float u1 = (b3 ? a[3] : a[2]) + dpp<ROR8>(b3 ? a[2] : a[3]);
    float v = (b2 ? u1 : u0) + dpp<HMIR>(b2 ? u0 : u1);
    v += dpp<XOR1>(v); v += dpp<XOR2>(v);
    v = rbf(rbf(v) * 0.08838834764831845f);
    if ((lane & 3) == 0) scw[it * 16] = v;
  };
  kblock(0, rK, (8 + w) * 4096, false);
  SB();
  float y[10];
  unsigned wp[8][10];
  {
    float v = fsum + dpp<ROR8>(fsum);
    v = rbf(xrow_sum(v));
    const float gs = rbf(v / (1.f + exp_(-v)));
    const float hh = gs * dpp<SHL4>(v);
    const unsigned hp = pk(hh, dpp<XOR1>(hh));
    const unsigned h01 = __builtin_amdgcn_readlane(hp, 0), h23 = __builtin_amdgcn_readlane(hp, 2);
#pragma unroll
    for (int k = 0; k < 4; ++k) {
      y[2 * k] = dot2(plo(da[2][k], da[3][k]), h23, dot2(plo(da[0][k], da[1][k]), h01, 0.f));
      y[2 * k + 1] = dot2(phi(da[2][k], da[3][k]), h23, dot2(phi(da[0][k], da[1][k]), h01, 0.f));
    }
    y[8] = dot2(plo(db[2], db[3]), h23, dot2(plo(db[0], db[1]), h01, 0.f));
    y[9] = dot2(phi(db[2], db[3]), h23, dot2(phi(db[0], db[1]), h01, 0.f));
#pragma unroll
    for (int i = 0; i < 8; ++i) {
#pragma unroll
      for (int k = 0; k < 4; ++k) { wp[i][2 * k] = plo(oa[2 * i][k], oa[2 * i + 1][k]); wp[i][2 * k + 1] = phi(oa[2 * i][k], oa[2 * i + 1][k]); }
      wp[i][8] = plo(ob[2 * i], ob[2 * i + 1]); wp[i][9] = phi(ob[2 * i], ob[2 * i + 1]);
#pragma unroll
      for (int k = 0; k < 10; ++k) asm volatile("" : "+v"(wp[i][k]));
    }
#pragma unroll
    for (int k = 0; k < 10; ++k) asm volatile("" : "+v"(y[k]));
  }
  SB();
  STAMP(3);
#pragma unroll 1
  for (int it = 1; it < 15; ++it) kblock(it, rK, ((it + 1) * 8 + w) * 4096, false);
  kblock(15, rV, w * 4096, true);
  SB();
  STAMP(4);

  const unsigned tagA = ep[0] + 1u, tagB = ep[1] + 1u;
  float mw;
  CB();
  {
    const f32x4 s4 = *(const f32x4*)&sm.sc[w][lane * 4];
    mw = wave_max(fmaxf(fmaxf(s4[0], s4[1]), fmaxf(s4[2], s4[3])));
    const float p0 = exp_(s4[0] - mw), p1 = exp_(s4[1] - mw), p2 = exp_(s4[2] - mw), p3 = exp_(s4[3] - mw);
    const unsigned q01 = pk(p0, p1), q23 = pk(p2, p3);
    u32x4 pw = {q01 & 0xffffu, q01 >> 16, q23 & 0xffffu, q23 >> 16};
    *(u32x4*)&sm.sc[w][lane * 4] = pw;
    const float lw = wave_sum((p0 + p1) + (p2 + p3));
    if (lane == 0) {
      u32x4 g = {f2u(mw), tagA, f2u(lw), tagB};
      __builtin_amdgcn_raw_buffer_store_b128(g, rWs, 0, WS_ML + (b * H + h) * 256 + (half * 8 + w) * 16, SC1);
    }
  }
  CB();
  float acc[8] = {0.f, 0.f, 0.f, 0.f, 0.f, 0.f, 0.f, 0.f};
  const unsigned* pr = (const unsigned*)&sm.sc[w][r * 4];
  auto vblock = [&](int it, bool more) __attribute__((always_inline)) {
    const u32x4 pv = *(const u32x4*)(pr + it * 16);
#pragma unroll
    for (int j = 0; j < 4; ++j) {
      const unsigned pl = pv[j], ph = pl << 16;
#pragma unroll
      for (int k = 0; k < 4; ++k) { acc[2 * k] = dot2(buf[j][k], pl, acc[2 * k]); acc[2 * k + 1] = dot2(buf[j][k], ph, acc[2 * k + 1]); }
      RSB();
      if (more) buf[j] = LD128(rV, kvo + j * 1024, ((it + 1) * 8 + w) * 4096, V_AUX);
      RSB();
    }
#pragma unroll
    for (int k = 0; k < 8; ++k) asm volatile("" : "+v"(acc[k]));
  };
#pragma unroll 1
  for (int it = 0; it < 13; ++it) vblock(it, true);
  SB();
  u32x4 ml = LD128(rWs, c * 16, WS_ML + (b * H + h) * 256, SC1);
  SB();
  vblock(13, true);
  SB();
  vblock(14, true);
  SB();
  float fw;
  {
    if (__builtin_expect(!__all(ml[1] == tagA && ml[3] == tagB), 0)) {
      unsigned spins = 0;
      do {
        __builtin_amdgcn_s_sleep(2);
        CB();
        ml = LD128(rWs, c * 16, WS_ML + (b * H + h) * 256, SC1);
      } while (!__all(ml[1] == tagA && ml[3] == tagB) && ++spins < SPIN_CAP);
    }
    const float mi = u2f(ml[0]), M = row_max(mi);
    const float L = row_sum(u2f(ml[2]) * exp_(mi - M));
    fw = exp_(mw - M) / L;
  }
  SB();
  vblock(15, false);
  STAMP(5);
  {
    float e[4];
#pragma unroll
    for (int k = 0; k < 4; ++k) e[k] = swap32_add(acc[k], acc[k + 4]);
    f32x2 g = {swap16_add(e[0], e[2]) * fw, swap16_add(e[1], e[3]) * fw};
    *(f32x2*)&sm.opart[w][8 * c + 2 * r] = g;
  }
  BARRIER();
  {
    float v = sm.opart[2 * r][16 * w + c] + sm.opart[2 * r + 1][16 * w + c];
    v = xrow_sum(v);
    const unsigned op = pk(v, dpp<XOR1>(v));
#pragma unroll
    for (int i = 0; i < 8; ++i) {
      const unsigned oi = __builtin_amdgcn_readlane(op, 2 * i);
#pragma unroll
      for (int k = 0; k < 10; ++k) y[k] = dot2(wp[i][k], oi, y[k]);
    }
    f32x4 y0 = {y[0], y[1], y[2], y[3]}, y1 = {y[4], y[5], y[6], y[7]};
    f32x2 y2 = {y[8], y[9]};
    *(f32x4*)&sm.ypart[w][lane * 4] = y0;
    *(f32x4*)&sm.ypart[w][256 + lane * 4] = y1;
    *(f32x2*)&sm.ypart[w][512 + lane * 2] = y2;
  }
  BARRIER();
  if (w < 5) {
    f32x2 s = *(const f32x2*)&sm.ypart[0][2 * tid];
#pragma unroll
    for (int i = 1; i < 8; ++i) s += *(const f32x2*)&sm.ypart[i][2 * tid];
    u32x4 g = {f2u(s[0]), tagA, f2u(s[1]), tagB};
    __builtin_amdgcn_raw_buffer_store_b128(g, rWs, tid * 16, WS_Y + (b * 64 + h * 2 + half) * 5120, SC1);
  }
  STAMP(6);
  if (!is_red) return;
  {
    u32x4 g[8];
    const unsigned so = WS_Y + (b * 64 + w * 8) * 5120 + rj * 1024;
#pragma unroll
    for (int i = 0; i < 8; ++i) g[i] = LD128(rWs, lane * 16, so + i * 5120, SC1);
    unsigned spins = 0;
    for (;;) {
      bool all = true;
      CB();
#pragma unroll
      for (int i = 0; i < 8; ++i) {
        if (!__all(g[i][1] == tagA && g[i][3] == tagB)) { all = false; g[i] = LD128(rWs, lane * 16, so + i * 5120, SC1); }
      }
      if (all || ++spins > SPIN_CAP) break;
      __builtin_amdgcn_s_sleep(1);
    }
    f32x2 s = {0.f, 0.f};
#pragma unroll
    for (int i = 0; i < 8; ++i) { s[0] += u2f(g[i][0]); s[1] += u2f(g[i][2]); }
    *(f32x2*)&sm.red[w][2 * lane] = s;
  }
  BARRIER();
  if (w == 0) {
    f32x2 s = *(const f32x2*)&sm.red[0][2 * lane];
#pragma unroll
    for (int i = 1; i < 8; ++i) s += *(const f32x2*)&sm.red[i][2 * lane];
    *(unsigned*)(out + b * D + ocol) = pk(blo(xo) + s[0], bhi(xo) + s[1]);
    if (rj == 0 && lane == 0) {
      u32x2 e = {tagA, tagB};
      __builtin_amdgcn_raw_buffer_store_b64(e, rWs, 0, WS_EPOCH + b * 256, SC1);
    }
  }
  STAMP(7);
}

extern "C" void kernel_launch(void* const* d_in, const int* in_sizes, int n_in, void* d_out, int out_size, void* d_ws, size_t ws_size,
                              hipStream_t stream) {
  fused_decode<<<256, 512, 0, stream>>>((const unsigned short*)d_in[0], (const unsigned short*)d_in[1], (const unsigned short*)d_in[2],
                                        (const unsigned short*)d_in[3], (const unsigned short*)d_in[4], (const unsigned short*)d_in[6],
                                        (const unsigned short*)d_in[7], (const unsigned short*)d_in[8], (unsigned short*)d_out,
                                        (unsigned char*)d_ws);
}
```

```cpp
#include <hip/hip_runtime.h>
#include <hip/hip_bf16.h>
#include <stdint.h>

typedef unsigned u32x4 __attribute__((ext_vector_type(4)));
typedef unsigned u32x2 __attribute__((ext_vector_type(2)));
typedef float f32x4 __attribute__((ext_vector_type(4)));
typedef float f32x2 __attribute__((ext_vector_type(2)));
typedef __bf16 bf16x2 __attribute__((ext_vector_type(2)));

#define W_AUX 0
#define K_AUX 2
#define V_AUX 2
constexpr int SC1 = 16;
constexpr int D = 640, H = 32, F = 1728;
constexpr unsigned WS_EPOCH = 0, WS_ML = 4096, WS_Y = 65536, WS_BYTES = WS_Y + 4 * 64 * 5120;
constexpr unsigned SPIN_CAP = 1u << 18;

#define LD128(r, vo, so, aux) __builtin_amdgcn_raw_buffer_load_b128(r, vo, so, aux)
#define LD64(r, vo, so, aux) __builtin_amdgcn_raw_buffer_load_b64(r, vo, so, aux)
#define LD32(r, vo, so, aux) __builtin_amdgcn_raw_buffer_load_b32(r, vo, so, aux)
#define SB() __builtin_amdgcn_sched_barrier(0)
#define RING 1
#define RSB() __builtin_amdgcn_sched_barrier(0)
#define CB() asm volatile("" ::: "memory")
#define BARRIER() asm volatile("s_waitcnt lgkmcnt(0)\n\ts_barrier" ::: "memory")

__device__ __forceinline__ float u2f(unsigned u) { return __builtin_bit_cast(float, u); }
__device__ __forceinline__ unsigned f2u(float f) { return __builtin_bit_cast(unsigned, f); }
__device__ __forceinline__ float blo(unsigned u) { return u2f(u << 16); }
__device__ __forceinline__ float bhi(unsigned u) { return u2f(u & 0xffff0000u); }
__device__ __forceinline__ unsigned pk(float lo, float hi) {
  f32x2 v = {lo, hi};
  return __builtin_bit_cast(unsigned, __builtin_convertvector(v, bf16x2));
}
__device__ __forceinline__ float rbf(float f) { return blo(pk(f, f)); }
__device__ __forceinline__ float dot2(unsigned a, unsigned b, float acc) {
  return __builtin_amdgcn_fdot2_f32_bf16(__builtin_bit_cast(bf16x2, a), __builtin_bit_cast(bf16x2, b), acc, false);
}
template <int CTRL> __device__ __forceinline__ float dpp(float x) {
  return u2f((unsigned)__builtin_amdgcn_mov_dpp((int)f2u(x), CTRL, 0xf, 0xf, true));
}
constexpr int XOR1 = 0xB1, XOR2 = 0x4E, HMIR = 0x141, ROR8 = 0x128, ROR4 = 0x124, SHL4 = 0x104;
__device__ __forceinline__ float swap32_add(float a, float b) {
  auto t = __builtin_amdgcn_permlane32_swap(f2u(a), f2u(b), false, false);
  return u2f(t[0]) + u2f(t[1]);
}
__device__ __forceinline__ float swap16_add(float a, float b) {
  auto t = __builtin_amdgcn_permlane16_swap(f2u(a), f2u(b), false, false);
  return u2f(t[0]) + u2f(t[1]);
}
__device__ __forceinline__ float xrow_sum(float x) { x = swap16_add(x, x); return swap32_add(x, x); }
__device__ __forceinline__ float xrow_max(float x) {
  auto s = __builtin_amdgcn_permlane16_swap(f2u(x), f2u(x), false, false);
  x = fmaxf(u2f(s[0]), u2f(s[1]));
  auto t = __builtin_amdgcn_permlane32_swap(f2u(x), f2u(x), false, false);
  return fmaxf(u2f(t[0]), u2f(t[1]));
}
__device__ __forceinline__ float row_sum(float v) { v += dpp<XOR1>(v); v += dpp<XOR2>(v); v += dpp<HMIR>(v); v += dpp<ROR8>(v); return v; }
__device__ __forceinline__ float row_max(float v) {
  v = fmaxf(v, dpp<XOR1>(v)); v = fmaxf(v, dpp<XOR2>(v)); v = fmaxf(v, dpp<HMIR>(v)); v = fmaxf(v, dpp<ROR8>(v)); return v;
}
__device__ __forceinline__ float wave_sum(float v) { return xrow_sum(row_sum(v)); }
__device__ __forceinline__ float wave_max(float v) { return xrow_max(row_max(v)); }
__device__ __forceinline__ unsigned plo(unsigned a, unsigned b) { return __builtin_amdgcn_perm(b, a, 0x05040100u); }
__device__ __forceinline__ unsigned phi(unsigned a, unsigned b) { return __builtin_amdgcn_perm(b, a, 0x07060302u); }
__device__ __forceinline__ float exp_(float x) { return __builtin_amdgcn_exp2f(x * 1.4426950408889634f); }

#define STAMP(i) do { } while (0)

struct Smem {
  unsigned xn[8][320];
  float qpart[8][128];
  float fpart[8][64];
  unsigned qv[8][64];
  float sc[8][256];
  float opart[8][128];
  float ypart[8][640];
  float red[8][128];
};

__global__ void __launch_bounds__(512, 2)
fused_decode(const unsigned short* __restrict__ x, const unsigned short* __restrict__ kc, const unsigned short* __restrict__ vc,
             const unsigned short* __restrict__ wq, const unsigned short* __restrict__ wo, const unsigned short* __restrict__ wg,
             const unsigned short* __restrict__ wu, const unsigned short* __restrict__ wd, unsigned short* __restrict__ out,
             unsigned char* ws) {
  __shared__ __attribute__((aligned(16))) Smem sm;
  const int tid = threadIdx.x, lane = tid & 63;
  const int w = __builtin_amdgcn_readfirstlane(tid >> 6);
  const int bid = blockIdx.x, x8 = bid & 7, rr_ = bid >> 3, half = rr_ & 1, b = (rr_ >> 1) & 3, hhi = rr_ >> 3, h = hhi * 8 + x8;
  const int r = lane >> 4, c = lane & 15;
  STAMP(0);
  const unsigned long long T0 = __builtin_amdgcn_s_memrealtime();
#define PADTO(us) do { while (__builtin_amdgcn_s_memrealtime() - T0 < (us) * 100ull) __builtin_amdgcn_s_sleep(1); } while (0)
  asm volatile("" :: "s"(x), "s"(kc), "s"(vc), "s"(wq), "s"(wo), "s"(wg), "s"(wu), "s"(wd), "s"(out), "s"(ws));

  const unsigned region = (unsigned)((b * H + h) * 2 + half);
  auto rX = __builtin_amdgcn_make_buffer_rsrc((void*)x, 0, 4 * D * 2, 0x00020000);
  auto rK = __builtin_amdgcn_make_buffer_rsrc((void*)(kc + (size_t)region * (2048 * 128)), 0, 512 * 1024, 0x00020000);
  auto rV = __builtin_amdgcn_make_buffer_rsrc((void*)(vc + (size_t)region * (2048 * 128)), 0, 512 * 1024, 0x00020000);
  auto rWq = __builtin_amdgcn_make_buffer_rsrc((void*)wq, 0, D * 4096 * 2, 0x00020000);
  auto rWo = __builtin_amdgcn_make_buffer_rsrc((void*)wo, 0, D * 4096 * 2, 0x00020000);
  auto rWg = __builtin_amdgcn_make_buffer_rsrc((void*)wg, 0, D * F * 2, 0x00020000);
  auto rWu = __builtin_amdgcn_make_buffer_rsrc((void*)wu, 0, D * F * 2, 0x00020000);
  auto rWs = __builtin_amdgcn_make_buffer_rsrc((void*)ws, 0, WS_BYTES, 0x00020000);
  const int slot = hhi * 2 + half, nch = slot < 3 ? 4 : 3, fcol = x8 * 216 + 8 * (slot * 3 + (slot < 3 ? slot : 3));
  auto rWd = __builtin_amdgcn_make_buffer_rsrc((void*)wd, 0, (w < 2 * nch) ? F * D * 2 : 0, 0x00020000);
  const int rj = (h - b * 5) & 31;
  const bool is_red = (half == 0) && (rj < 5);
  const int gp = rj * 64 + lane;
  const int ocol = gp < 128 ? 8 * (gp >> 1) + 2 * (gp & 1) : (gp < 256 ? 8 * ((gp - 128) >> 1) + 4 + 2 * (gp & 1) : 512 + 2 * (gp - 256));

  const u32x4 xa = LD128(rX, lane * 16, b * 1280, 0);
  const unsigned xb = LD32(rX, 1024 + lane * 4, b * 1280, 0);
  const unsigned xo = LD32(rX, is_red ? ocol * 2 : 0, b * 1280, 0);
  const u32x2 ep = LD64(rWs, 0, WS_EPOCH + b * 256, SC1);
  SB();
  u32x4 wqv[20];
  {
    const unsigned vo = r * (20 * 8192) + c * 16, so = w * (80 * 8192) + h * 256;
#pragma unroll
    for (int i = 0; i < 20; ++i) wqv[i] = LD128(rWq, vo, so + i * 8192, W_AUX);
  }
  SB();
  u32x4 gv[5], uv[5];
  {
    const unsigned vo = (lane & 3) < nch ? (lane >> 2) * (5 * 3456) + (lane & 3) * 16 : 0x80000000u, so = w * (80 * 3456) + fcol * 2;
#pragma unroll
    for (int i = 0; i < 5; ++i) { gv[i] = LD128(rWg, vo, so + i * 3456, W_AUX); uv[i] = LD128(rWu, vo, so + i * 3456, W_AUX); }
  }
  SB();
  u32x4 buf[4];
  const unsigned kvo = lane * 16;
#pragma unroll
  for (int j = 0; j < 4; ++j) buf[j] = LD128(rK, kvo + j * 1024, w * 4096, K_AUX);
  SB();
  u32x4 da[4]; unsigned db[4];
#pragma unroll
  for (int i = 0; i < 4; ++i) { const unsigned so = (fcol + 4 * w + i) * 1280; da[i] = LD128(rWd, lane * 16, so, W_AUX); db[i] = LD32(rWd, 1024 + lane * 4, so, W_AUX); }
  SB();
  u32x4 oa[16]; unsigned ob[16];
#pragma unroll
  for (int i = 0; i < 8; ++i) { const unsigned so = (h * 128 + 16 * w + i) * 1280; oa[i] = LD128(rWo, lane * 16, so, W_AUX); ob[i] = LD32(rWo, 1024 + lane * 4, so, W_AUX); }
  SB();

  {
    float v[10] = {blo(xa[0]), bhi(xa[0]), blo(xa[1]), bhi(xa[1]), blo(xa[2]), bhi(xa[2]), blo(xa[3]), bhi(xa[3]), blo(xb), bhi(xb)};
    float s = 0.f;
#pragma unroll
    for (int i = 0; i < 10; ++i) s += v[i];
    const float mean = wave_sum(s) * (1.f / D);
    float s2 = 0.f;
#pragma unroll
    for (int i = 0; i < 10; ++i) { const float d = v[i] - mean; s2 += d * d; }
    const float var = wave_sum(s2) * (1.f / D);
    const float mb = rbf(mean), den = rbf(__builtin_sqrtf(rbf(rbf(var) + 1e-5f))), rd = 1.f / den;
#pragma unroll
    for (int i = 0; i < 10; ++i) v[i] = rbf(v[i] - mb) * rd;
    u32x4 o4 = {pk(v[0], v[1]), pk(v[2], v[3]), pk(v[4], v[5]), pk(v[6], v[7])};
    *(u32x4*)&sm.xn[w][lane * 4] = o4;
    sm.xn[w][256 + lane] = pk(v[8], v[9]);
  }
  CB();
  {
    unsigned xp[10];
    const unsigned* xs = &sm.xn[w][40 * w + 10 * r];
#pragma unroll
    for (int i = 0; i < 5; ++i) { const u32x2 t = *(const u32x2*)(xs + 2 * i); xp[2 * i] = t[0]; xp[2 * i + 1] = t[1]; }
    float a[8] = {0.f, 0.f, 0.f, 0.f, 0.f, 0.f, 0.f, 0.f};
#pragma unroll
    for (int i = 0; i < 10; ++i)
#pragma unroll
      for (int k = 0; k < 4; ++k) {
        a[2 * k] = dot2(plo(wqv[2 * i][k], wqv[2 * i + 1][k]), xp[i], a[2 * k]);
        a[2 * k + 1] = dot2(phi(wqv[2 * i][k], wqv[2 * i + 1][k]), xp[i], a[2 * k + 1]);
      }
    float e[4];
#pragma unroll
    for (int k = 0; k < 4; ++k) e[k] = swap32_add(a[k], a[k + 4]);
    f32x2 g = {swap16_add(e[0], e[2]), swap16_add(e[1], e[3])};
    *(f32x2*)&sm.qpart[w][8 * c + 2 * r] = g;
  }
  SB();
#pragma unroll
  for (int i = 8; i < 16; ++i) { const unsigned so = (h * 128 + 16 * w + i) * 1280; oa[i] = LD128(rWo, lane * 16, so, W_AUX); ob[i] = LD32(rWo, 1024 + lane * 4, so, W_AUX); }
  SB();
  {
    const unsigned short* xs = (const unsigned short*)&sm.xn[w][0] + 80 * w + 5 * (lane >> 2);
    float a[16];
#pragma unroll
    for (int k = 0; k < 16; ++k) a[k] = 0.f;
#pragma unroll
    for (int i = 0; i < 5; ++i) {
      const unsigned xl = xs[i], xh = xl << 16;
#pragma unroll
      for (int k = 0; k < 4; ++k) {
        a[2 * k] = dot2(gv[i][k], xl, a[2 * k]); a[2 * k + 1] = dot2(gv[i][k], xh, a[2 * k + 1]);
        a[8 + 2 * k] = dot2(uv[i][k], xl, a[8 + 2 * k]); a[9 + 2 * k] = dot2(uv[i][k], xh, a[9 + 2 * k]);
      }
    }
    float e[8];
#pragma unroll
    for (int k = 0; k < 8; ++k) e[k] = swap32_add(a[k], a[k + 8]);
    f32x4 f;
#pragma unroll
    for (int k = 0; k < 4; ++k) { float t = swap16_add(e[k], e[k + 4]); t += dpp<ROR4>(t); t += dpp<ROR8>(t); f[k] = t; }
    if ((lane & 12) == 0) *(f32x4*)&sm.fpart[w][(lane >> 5) * 32 + (lane & 3) * 8 + ((lane >> 4) & 1) * 4] = f;
  }
  STAMP(1);
  BARRIER();
  u32x4 qp;
  {
    f32x2 s = *(const f32x2*)&sm.qpart[0][2 * lane];
#pragma unroll
    for (int i = 1; i < 8; ++i) s += *(const f32x2*)&sm.qpart[i][2 * lane];
    sm.qv[w][lane] = pk(s[0], s[1]);
    CB();
    qp = *(const u32x4*)&sm.qv[w][4 * c];
  }
  const float fsum = sm.fpart[lane >> 3][((lane >> 2) & 1) * 32 + 4 * w + (lane & 3)];
  STAMP(2);

  const bool b3 = lane & 8, b2 = lane & 4;
  float* scw = &sm.sc[w][r * 4 + ((lane >> 3) & 1) + ((lane >> 1) & 2)];
  auto kblock = [&](int it, auto rsrc, unsigned so, bool isv) __attribute__((always_inline)) {
    float a[4];
#pragma unroll
    for (int j = 0; j < 4; ++j) {
      float t = dot2(buf[j][0], qp[0], 0.f); t = dot2(buf[j][1], qp[1], t); t = dot2(buf[j][2], qp[2], t); a[j] = dot2(buf[j][3], qp[3], t);
      RSB();
      buf[j] = isv ? LD128(rsrc, kvo + j * 1024, so, V_AUX) : LD128(rsrc, kvo + j * 1024, so, K_AUX);
      RSB();
    }
    const float u0 = (b3 ? a[1] : a[0]) + dpp<ROR8>(b3 ? a[0] : a[1]);
    const float u1 = (b3 ? a[3] : a[2]) + dpp<ROR8>(b3 ? a[2] : a[3]);
    float v = (b2 ? u1 : u0) + dpp<HMIR>(b2 ? u0 : u1);
    v += dpp<XOR1>(v); v += dpp<XOR2>(v);
    v = rbf(rbf(v) * 0.08838834764831845f);
    if ((lane & 3) == 0) scw[it * 16] = v;
  };
  kblock(0, rK, (8 + w) * 4096, false);
  SB();
  float y[10];
  unsigned wp[8][10];
  {
    float v = fsum + dpp<ROR8>(fsum);
    v = rbf(xrow_sum(v));
    const float gs = rbf(v / (1.f + exp_(-v)));
    const float hh = gs * dpp<SHL4>(v);
    const unsigned hp = pk(hh, dpp<XOR1>(hh));
    const unsigned h01 = __builtin_amdgcn_readlane(hp, 0), h23 = __builtin_amdgcn_readlane(hp, 2);
#pragma unroll
    for (int k = 0; k < 4; ++k) {
      y[2 * k] = dot2(plo(da[2][k], da[3][k]), h23, dot2(plo(da[0][k], da[1][k]), h01, 0.f));
      y[2 * k + 1] = dot2(phi(da[2][k], da[3][k]), h23, dot2(phi(da[0][k], da[1][k]), h01, 0.f));
    }
    y[8] = dot2(plo(db[2], db[3]), h23, dot2(plo(db[0], db[1]), h01, 0.f));
    y[9] = dot2(phi(db[2], db[3]), h23, dot2(phi(db[0], db[1]), h01, 0.f));
#pragma unroll
    for (int i = 0; i < 8; ++i) {
#pragma unroll
      for (int k = 0; k < 4; ++k) { wp[i][2 * k] = plo(oa[2 * i][k], oa[2 * i + 1][k]); wp[i][2 * k + 1] = phi(oa[2 * i][k], oa[2 * i + 1][k]); }
      wp[i][8] = plo(ob[2 * i], ob[2 * i + 1]); wp[i][9] = phi(ob[2 * i], ob[2 * i + 1]);
#pragma unroll
      for (int k = 0; k < 10; ++k) asm volatile("" : "+v"(wp[i][k]));
    }
#pragma unroll
    for (int k = 0; k < 10; ++k) asm volatile("" : "+v"(y[k]));
  }
  SB();
  STAMP(3);
#pragma unroll 1
  for (int it = 1; it < 15; ++it) kblock(it, rK, ((it + 1) * 8 + w) * 4096, false);
  kblock(15, rV, w * 4096, true);
  SB();
  STAMP(4);

  const unsigned tagA = ep[0] + 1u, tagB = ep[1] + 1u;
  float mw;
  CB();
  {
    const f32x4 s4 = *(const f32x4*)&sm.sc[w][lane * 4];
    mw = wave_max(fmaxf(fmaxf(s4[0], s4[1]), fmaxf(s4[2], s4[3])));
    const float p0 = exp_(s4[0] - mw), p1 = exp_(s4[1] - mw), p2 = exp_(s4[2] - mw), p3 = exp_(s4[3] - mw);
    const unsigned q01 = pk(p0, p1), q23 = pk(p2, p3);
    u32x4 pw = {q01 & 0xffffu, q01 >> 16, q23 & 0xffffu, q23 >> 16};
    *(u32x4*)&sm.sc[w][lane * 4] = pw;
    const float lw = wave_sum((p0 + p1) + (p2 + p3));
    if (lane == 0) {
      u32x4 g = {f2u(mw), tagA, f2u(lw), tagB};
      __builtin_amdgcn_raw_buffer_store_b128(g, rWs, 0, WS_ML + (b * H + h) * 256 + (half * 8 + w) * 16, SC1);
    }
  }
  CB();
  float acc[8] = {0.f, 0.f, 0.f, 0.f, 0.f, 0.f, 0.f, 0.f};
  const unsigned* pr = (const unsigned*)&sm.sc[w][r * 4];
  auto vblock = [&](int it, bool more) __attribute__((always_inline)) {
    const u32x4 pv = *(const u32x4*)(pr + it * 16);
#pragma unroll
    for (int j = 0; j < 4; ++j) {
      const unsigned pl = pv[j], ph = pl << 16;
#pragma unroll
      for (int k = 0; k < 4; ++k) { acc[2 * k] = dot2(buf[j][k], pl, acc[2 * k]); acc[2 * k + 1] = dot2(buf[j][k], ph, acc[2 * k + 1]); }
      RSB();
      if (more) buf[j] = LD128(rV, kvo + j * 1024, ((it + 1) * 8 + w) * 4096, V_AUX);
      RSB();
    }
#pragma unroll
    for (int k = 0; k < 8; ++k) asm volatile("" : "+v"(acc[k]));
  };
#pragma unroll 1
  for (int it = 0; it < 13; ++it) vblock(it, true);
  SB();
  u32x4 ml = LD128(rWs, c * 16, WS_ML + (b * H + h) * 256, SC1);
  SB();
  vblock(13, true);
  SB();
  vblock(14, true);
  SB();
  float fw;
  {
    if (__builtin_expect(!__all(ml[1] == tagA && ml[3] == tagB), 0)) {
      unsigned spins = 0;
      do {
        __builtin_amdgcn_s_sleep(2);
        CB();
        ml = LD128(rWs, c * 16, WS_ML + (b * H + h) * 256, SC1);
      } while (!__all(ml[1] == tagA && ml[3] == tagB) && ++spins < SPIN_CAP);
    }
    const float mi = u2f(ml[0]), M = row_max(mi);
    const float L = row_sum(u2f(ml[2]) * exp_(mi - M));
    fw = exp_(mw - M) / L;
  }
  SB();
  vblock(15, false);
  STAMP(5);
  {
    float e[4];
#pragma unroll
    for (int k = 0; k < 4; ++k) e[k] = swap32_add(acc[k], acc[k + 4]);
    f32x2 g = {swap16_add(e[0], e[2]) * fw, swap16_add(e[1], e[3]) * fw};
    *(f32x2*)&sm.opart[w][8 * c + 2 * r] = g;
  }
  BARRIER();
  {
    float v = sm.opart[2 * r][16 * w + c] + sm.opart[2 * r + 1][16 * w + c];
    v = xrow_sum(v);
    const unsigned op = pk(v, dpp<XOR1>(v));
#pragma unroll
    for (int i = 0; i < 8; ++i) {
      const unsigned oi = __builtin_amdgcn_readlane(op, 2 * i);
#pragma unroll
      for (int k = 0; k < 10; ++k) y[k] = dot2(wp[i][k], oi, y[k]);
    }
    f32x4 y0 = {y[0], y[1], y[2], y[3]}, y1 = {y[4], y[5], y[6], y[7]};
    f32x2 y2 = {y[8], y[9]};
    *(f32x4*)&sm.ypart[w][lane * 4] = y0;
    *(f32x4*)&sm.ypart[w][256 + lane * 4] = y1;
    *(f32x2*)&sm.ypart[w][512 + lane * 2] = y2;
  }
  BARRIER();
  if (w < 5) {
    f32x2 s = *(const f32x2*)&sm.ypart[0][2 * tid];
#pragma unroll
    for (int i = 1; i < 8; ++i) s += *(const f32x2*)&sm.ypart[i][2 * tid];
    u32x4 g = {f2u(s[0]), tagA, f2u(s[1]), tagB};
    __builtin_amdgcn_raw_buffer_store_b128(g, rWs, tid * 16, WS_Y + (b * 64 + h * 2 + half) * 5120, SC1);
  }
  STAMP(6);
  if (!is_red) { PADTO(80); return; }
  {
    u32x4 g[8];
    const unsigned so = WS_Y + (b * 64 + w * 8) * 5120 + rj * 1024;
#pragma unroll
    for (int i = 0; i < 8; ++i) g[i] = LD128(rWs, lane * 16, so + i * 5120, SC1);
    unsigned spins = 0;
    for (;;) {
      bool all = true;
      CB();
#pragma unroll
      for (int i = 0; i < 8; ++i) {
        if (!__all(g[i][1] == tagA && g[i][3] == tagB)) { all = false; g[i] = LD128(rWs, lane * 16, so + i * 5120, SC1); }
      }
      if (all || ++spins > SPIN_CAP) break;
      __builtin_amdgcn_s_sleep(1);
    }
    f32x2 s = {0.f, 0.f};
#pragma unroll
    for (int i = 0; i < 8; ++i) { s[0] += u2f(g[i][0]); s[1] += u2f(g[i][2]); }
    *(f32x2*)&sm.red[w][2 * lane] = s;
  }
  BARRIER();
  if (w == 0) {
    f32x2 s = *(const f32x2*)&sm.red[0][2 * lane];
#pragma unroll
    for (int i = 1; i < 8; ++i) s += *(const f32x2*)&sm.red[i][2 * lane];
    *(unsigned*)(out + b * D + ocol) = pk(blo(xo) + s[0], bhi(xo) + s[1]);
    if (rj == 0 && lane == 0) {
      u32x2 e = {tagA, tagB};
      __builtin_amdgcn_raw_buffer_store_b64(e, rWs, 0, WS_EPOCH + b * 256, SC1);
    }
  }
  PADTO(80);
}

__global__ void k_nop(unsigned char* ws) { if (threadIdx.x == 9999) ws[8 << 20] = 1; }

extern "C" void kernel_launch(void* const* d_in, const int* in_sizes, int n_in, void* d_out, int out_size, void* d_ws, size_t ws_size,
                              hipStream_t stream) {
  fused_decode<<<256, 512, 0, stream>>>((const unsigned short*)d_in[0], (const unsigned short*)d_in[1], (const unsigned short*)d_in[2],
                                        (const unsigned short*)d_in[3], (const unsigned short*)d_in[4], (const unsigned short*)d_in[6],
                                        (const unsigned short*)d_in[7], (const unsigned short*)d_in[8], (unsigned short*)d_out,
                                        (unsigned char*)d_ws);
  k_nop<<<1, 64, 0, stream>>>((unsigned char*)d_ws);
}
```

```cpp
#include <hip/hip_runtime.h>
#include <hip/hip_bf16.h>
#include <stdint.h>
#include <utility>

typedef unsigned u32x4 __attribute__((ext_vector_type(4)));
typedef unsigned u32x2 __attribute__((ext_vector_type(2)));
typedef float f32x4 __attribute__((ext_vector_type(4)));
typedef float f32x2 __attribute__((ext_vector_type(2)));
typedef __bf16 bf16x2 __attribute__((ext_vector_type(2)));

#define W_AUX 0
#define K_AUX 2
#define V_AUX 2
constexpr int SC1 = 16;
constexpr int D = 640, H = 32, F = 1728;
constexpr unsigned WS_EPOCH = 0, WS_ML = 4096, WS_Y = 65536, WS_BYTES = WS_Y + 4 * 64 * 5120;
constexpr unsigned SPIN_CAP = 1u << 18;

#define LD128(r, vo, so, aux) __builtin_amdgcn_raw_buffer_load_b128(r, vo, so, aux)
#define LD64(r, vo, so, aux) __builtin_amdgcn_raw_buffer_load_b64(r, vo, so, aux)
#define LD32(r, vo, so, aux) __builtin_amdgcn_raw_buffer_load_b32(r, vo, so, aux)
#define SB() __builtin_amdgcn_sched_barrier(0)
#define RING 1
#define RSB() __builtin_amdgcn_sched_barrier(0)
#define CB() asm volatile("" ::: "memory")
#define BARRIER() asm volatile("s_waitcnt lgkmcnt(0)\n\ts_barrier" ::: "memory")

__device__ __forceinline__ float u2f(unsigned u) { return __builtin_bit_cast(float, u); }
__device__ __forceinline__ unsigned f2u(float f) { return __builtin_bit_cast(unsigned, f); }
__device__ __forceinline__ float blo(unsigned u) { return u2f(u << 16); }
__device__ __forceinline__ float bhi(unsigned u) { return u2f(u & 0xffff0000u); }
__device__ __forceinline__ unsigned pk(float lo, float hi) {
  f32x2 v = {lo, hi};
  return __builtin_bit_cast(unsigned, __builtin_convertvector(v, bf16x2));
}
__device__ __forceinline__ float rbf(float f) { return blo(pk(f, f)); }
__device__ __forceinline__ float dot2(unsigned a, unsigned b, float acc) {
  return __builtin_amdgcn_fdot2_f32_bf16(__builtin_bit_cast(bf16x2, a), __builtin_bit_cast(bf16x2, b), acc, false);
}
template <int CTRL> __device__ __forceinline__ float dpp(float x) {
  return u2f((unsigned)__builtin_amdgcn_mov_dpp((int)f2u(x), CTRL, 0xf, 0xf, true));
}
constexpr int XOR1 = 0xB1, XOR2 = 0x4E, HMIR = 0x141, ROR8 = 0x128, ROR4 = 0x124, SHL4 = 0x104;
__device__ __forceinline__ float swap32_add(float a, float b) {
  auto t = __builtin_amdgcn_permlane32_swap(f2u(a), f2u(b), false, false);
  return u2f(t[0]) + u2f(t[1]);
}
__device__ __forceinline__ float swap16_add(float a, float b) {
  auto t = __builtin_amdgcn_permlane16_swap(f2u(a), f2u(b), false, false);
  return u2f(t[0]) + u2f(t[1]);
}
__device__ __forceinline__ float xrow_sum(float x) { x = swap16_add(x, x); return swap32_add(x, x); }
__device__ __forceinline__ float xrow_max(float x) {
  auto s = __builtin_amdgcn_permlane16_swap(f2u(x), f2u(x), false, false);
  x = fmaxf(u2f(s[0]), u2f(s[1]));
  auto t = __builtin_amdgcn_permlane32_swap(f2u(x), f2u(x), false, false);
  return fmaxf(u2f(t[0]), u2f(t[1]));
}
__device__ __forceinline__ float row_sum(float v) { v += dpp<XOR1>(v); v += dpp<XOR2>(v); v += dpp<HMIR>(v); v += dpp<ROR8>(v); return v; }
__device__ __forceinline__ float row_max(float v) {
  v = fmaxf(v, dpp<XOR1>(v)); v = fmaxf(v, dpp<XOR2>(v)); v = fmaxf(v, dpp<HMIR>(v)); v = fmaxf(v, dpp<ROR8>(v)); return v;
}
__device__ __forceinline__ float wave_sum(float v) { return xrow_sum(row_sum(v)); }
__device__ __forceinline__ float wave_max(float v) { return xrow_max(row_max(v)); }
__device__ __forceinline__ unsigned plo(unsigned a, unsigned b) { return __builtin_amdgcn_perm(b, a, 0x05040100u); }
__device__ __forceinline__ unsigned phi(unsigned a, unsigned b) { return __builtin_amdgcn_perm(b, a, 0x07060302u); }
__device__ __forceinline__ float exp_(float x) { return __builtin_amdgcn_exp2f(x * 1.4426950408889634f); }

#define STAMP(i) do { } while (0)

template <int N> struct IC { static constexpr int value = N; constexpr operator int() const { return N; } };
template <typename Fn, int... I> __device__ __forceinline__ void sfor_impl(Fn&& f, std::integer_sequence<int, I...>) { (f(IC<I>{}), ...); }
template <int N, typename Fn> __device__ __forceinline__ void sfor(Fn&& f) { sfor_impl(f, std::make_integer_sequence<int, N>{}); }
#define HMAP 1
#define PSEQ "wwwKwwwKwwwKwwwKwwwKwwwKwwK"
#define WN 8
#define XB 2
#define T_D2 32
#define T_D3 32
constexpr char pseq[] = PSEQ;
constexpr int PLEN = sizeof(pseq) - 1;
constexpr int pcount(int n, char ch) { int a = 0; for (int i = 0; i < n; ++i) a += pseq[i] == ch; return a; }
constexpr int PK = pcount(PLEN, 'K');
constexpr int PNL = 20 + 4 * PK;
static_assert(pcount(PLEN, 'w') == 20, "20 wq loads");
constexpr int pload(int n) { int k = 0; for (int i = 0; i < PLEN; ++i) { const int len = pseq[i] == 'w' ? 1 : 4; if (n < k + len) return pseq[i] == 'w' ? pcount(i, 'w') : -(1 + 4 * pcount(i, 'K') + (n - k)); k += len; } return 0; }
constexpr int wq_done(int n) { int a = 0; for (int i = 0; i < n && i < PNL; ++i) a += pload(i) >= 0; return a / 2; }
constexpr int icum(int t) {
  int v = PK + (XB >= 1 ? 1 : 0);
  if (XB >= 2 && t >= PK / 2) v = PK + 2;
  if (XB >= 3 && t >= PK - 1) v = PK + 3;
  const int d = t >= T_D3 ? 3 : (t >= T_D2 ? 2 : 1);
  if (t + 1 + d > v) v = t + 1 + d;
  return v > 32 ? 32 : v;
}
#define T_X0 (PK + XB - 1)
#define T_X1 29
#define GUW 8
constexpr int xstep(int i) { const int cw = i < 10 ? GUW * i : GUW * 10 + 5 * (i - 10); return T_X0 + cw * (T_X1 - T_X0) / (GUW * 10 + 100); }
constexpr int xcum(int t) { int a = 0; for (int i = 0; i < 30; ++i) a += xstep(i) < t; return a; }
constexpr int pcum(int t) { const int n = xcum(t - 2) - 14; return n < 0 ? 0 : n / 2; }
constexpr int T_FF = xstep(9) + 2;
constexpr int T_H = xstep(13) + 2 > T_FF + 3 ? xstep(13) + 2 : T_FF + 3;
constexpr int T_ML = 28;
static_assert(pcum(32) == 8 && T_H < 31, "schedule must finish inside the stream");
constexpr int vmcnt_imm(int n) { return (n & 15) | ((n >> 4) << 14) | 0x0F70; }

struct Smem {
  unsigned xn[8][320];
  float qpart[8][128];
  float fpart[8][64];
  unsigned qv[8][64];
  float sc[8][256];
  float opart[8][128];
  float ypart[8][640];
  float red[8][128];
  unsigned cnt;
};

__global__ void __launch_bounds__(512, 2)
fused_decode(const unsigned short* __restrict__ x, const unsigned short* __restrict__ kc, const unsigned short* __restrict__ vc,
             const unsigned short* __restrict__ wq, const unsigned short* __restrict__ wo, const unsigned short* __restrict__ wg,
             const unsigned short* __restrict__ wu, const unsigned short* __restrict__ wd, unsigned short* __restrict__ out,
             unsigned char* ws) {
  __shared__ __attribute__((aligned(16))) Smem sm;
  const int tid = threadIdx.x, lane = tid & 63;
  const int w = __builtin_amdgcn_readfirstlane(tid >> 6);
  const int bid = blockIdx.x, x8 = bid & 7, rr_ = bid >> 3, half = rr_ & 1, b = (rr_ >> 1) & 3, hhi = rr_ >> 3, h = HMAP ? x8 * 4 + hhi : hhi * 8 + x8;
  const int r = lane >> 4, c = lane & 15;
  STAMP(0);
  asm volatile("" :: "s"(x), "s"(kc), "s"(vc), "s"(wq), "s"(wo), "s"(wg), "s"(wu), "s"(wd), "s"(out), "s"(ws));

  const unsigned region = (unsigned)((b * H + h) * 2 + half);
  auto rX = __builtin_amdgcn_make_buffer_rsrc((void*)x, 0, 4 * D * 2, 0x00020000);
  auto rK = __builtin_amdgcn_make_buffer_rsrc((void*)(kc + (size_t)region * (2048 * 128)), 0, 512 * 1024, 0x00020000);
  auto rV = __builtin_amdgcn_make_buffer_rsrc((void*)(vc + (size_t)region * (2048 * 128)), 0, 512 * 1024, 0x00020000);
  auto rWq = __builtin_amdgcn_make_buffer_rsrc((void*)wq, 0, D * 4096 * 2, 0x00020000);
  auto rWo = __builtin_amdgcn_make_buffer_rsrc((void*)wo, 0, D * 4096 * 2, 0x00020000);
  auto rWs = __builtin_amdgcn_make_buffer_rsrc((void*)ws, 0, WS_BYTES, 0x00020000);
  const int slot = hhi * 2 + half, fcol = (x8 < 3 ? 4 * x8 : 3 * x8 + 3) * 64 + slot * 32;
  const bool has_ffn = slot < (x8 < 3 ? 8 : 6);
  auto rWg = __builtin_amdgcn_make_buffer_rsrc((void*)wg, 0, has_ffn ? D * F * 2 : 0, 0x00020000);
  auto rWu = __builtin_amdgcn_make_buffer_rsrc((void*)wu, 0, has_ffn ? D * F * 2 : 0, 0x00020000);
  auto rWd = __builtin_amdgcn_make_buffer_rsrc((void*)wd, 0, has_ffn ? F * D * 2 : 0, 0x00020000);
  const int rj = (h - b * 5) & 31;
  const bool is_red = (half == 0) && (rj < 5);
  const int gp = rj * 64 + lane;
  const int ocol = gp < 128 ? 8 * (gp >> 1) + 2 * (gp & 1) : (gp < 256 ? 8 * ((gp - 128) >> 1) + 4 + 2 * (gp & 1) : 512 + 2 * (gp - 256));

  const u32x4 xa = LD128(rX, lane * 16, b * 1280, 0);
  const unsigned xb = LD32(rX, 1024 + lane * 4, b * 1280, 0);
  const unsigned xo = LD32(rX, is_red ? ocol * 2 : 0, b * 1280, 0);
  const u32x2 ep = LD64(rWs, 0, WS_EPOCH + b * 256, SC1);
  if (tid == 0) sm.cnt = 0;
  SB();
  u32x4 wqv[20];
  u32x4 kv[33][4];
  const unsigned kvo = lane * 16;
  const unsigned wq_vo = r * (20 * 8192) + c * 16, wq_so = w * (80 * 8192) + h * 256;
  auto ld_kv1 = [&](auto T, auto J) __attribute__((always_inline)) {
    constexpr int t = T, j = J;
    if constexpr (t < 16) kv[t][j] = LD128(rK, kvo + j * 1024, (t * 8 + w) * 4096, K_AUX);
    else if constexpr (t < 32) kv[t][j] = LD128(rV, kvo + j * 1024, ((t - 16) * 8 + w) * 4096, V_AUX);
    SB();
  };
  auto ld_kv = [&](auto T) __attribute__((always_inline)) { sfor<4>([&](auto J) __attribute__((always_inline)) { ld_kv1(T, J); }); };
  auto ld_pro = [&](auto N) __attribute__((always_inline)) {
    constexpr int p = pload(N);
    if constexpr (p >= 0) { wqv[p] = LD128(rWq, wq_vo, wq_so + p * 8192, W_AUX); SB(); }
    else ld_kv1(IC<(-p - 1) / 4>{}, IC<(-p - 1) % 4>{});
  };
  sfor<WN>([&](auto N) __attribute__((always_inline)) { ld_pro(N); });
  {
    float v[10] = {blo(xa[0]), bhi(xa[0]), blo(xa[1]), bhi(xa[1]), blo(xa[2]), bhi(xa[2]), blo(xa[3]), bhi(xa[3]), blo(xb), bhi(xb)};
    float s = 0.f;
#pragma unroll
    for (int i = 0; i < 10; ++i) s += v[i];
    const float mean = wave_sum(s) * (1.f / D);
    float s2 = 0.f;
#pragma unroll
    for (int i = 0; i < 10; ++i) { const float d = v[i] - mean; s2 += d * d; }
    const float var = wave_sum(s2) * (1.f / D);
    const float mb = rbf(mean), den = rbf(__builtin_sqrtf(rbf(rbf(var) + 1e-5f))), rd = 1.f / den;
#pragma unroll
    for (int i = 0; i < 10; ++i) v[i] = rbf(v[i] - mb) * rd;
    u32x4 o4 = {pk(v[0], v[1]), pk(v[2], v[3]), pk(v[4], v[5]), pk(v[6], v[7])};
    *(u32x4*)&sm.xn[w][lane * 4] = o4;
    sm.xn[w][256 + lane] = pk(v[8], v[9]);
  }
  CB();
  {
    unsigned xp[10];
    const unsigned* xs = &sm.xn[w][40 * w + 10 * r];
#pragma unroll
    for (int i = 0; i < 5; ++i) { const u32x2 t = *(const u32x2*)(xs + 2 * i); xp[2 * i] = t[0]; xp[2 * i + 1] = t[1]; }
    float a[8] = {0.f, 0.f, 0.f, 0.f, 0.f, 0.f, 0.f, 0.f};
    SB();
    auto qpair = [&](auto I) __attribute__((always_inline)) {
      constexpr int i = I;
#pragma unroll
      for (int k = 0; k < 4; ++k) {
        a[2 * k] = dot2(plo(wqv[2 * i][k], wqv[2 * i + 1][k]), xp[i], a[2 * k]);
        a[2 * k + 1] = dot2(phi(wqv[2 * i][k], wqv[2 * i + 1][k]), xp[i], a[2 * k + 1]);
      }
      SB();
    };
    sfor<PNL - WN>([&](auto I) __attribute__((always_inline)) {
      constexpr int n = I + WN;
      __builtin_amdgcn_s_waitcnt(vmcnt_imm(WN - 1));
      SB();
      ld_pro(IC<n>{});
      sfor<wq_done(n - WN + 1) - wq_done(n - WN)>([&](auto J) __attribute__((always_inline)) { qpair(IC<wq_done(n - WN) + J>{}); });
    });
    sfor<10 - wq_done(PNL - WN)>([&](auto J) __attribute__((always_inline)) { qpair(IC<wq_done(PNL - WN) + J>{}); });
    float e[4];
#pragma unroll
    for (int k = 0; k < 4; ++k) e[k] = swap32_add(a[k], a[k + 4]);
    f32x2 g = {swap16_add(e[0], e[2]), swap16_add(e[1], e[3])};
    *(f32x2*)&sm.qpart[w][8 * c + 2 * r] = g;
  }
  STAMP(1);
  BARRIER();
  sfor<icum(-1) - PK>([&](auto I) __attribute__((always_inline)) { ld_kv(IC<PK + I>{}); });
  u32x4 qp;
  {
    f32x2 s = *(const f32x2*)&sm.qpart[0][2 * lane];
#pragma unroll
    for (int i = 1; i < 8; ++i) s += *(const f32x2*)&sm.qpart[i][2 * lane];
    sm.qv[w][lane] = pk(s[0], s[1]);
    CB();
    qp = *(const u32x4*)&sm.qv[w][4 * c];
  }
  STAMP(2);

  const bool b3 = lane & 8, b2 = lane & 4;
  float* scw = &sm.sc[w][r * 4 + ((lane >> 3) & 1) + ((lane >> 1) & 2)];
  const unsigned* pr = (const unsigned*)&sm.sc[w][r * 4];
  const unsigned gu_vo = (lane >> 2) * (5 * 3456) + (lane & 3) * 16, gu_so = w * (80 * 3456) + fcol * 2;
  u32x4 gv[5], uv[5];
  u32x4 ra[20]; unsigned rb[20];
  unsigned wp[8][10];
  float y[10];
  float acc[8] = {0.f, 0.f, 0.f, 0.f, 0.f, 0.f, 0.f, 0.f};
  float mw = 0.f, fw = 0.f;
  u32x4 ml;
  const unsigned tagA = ep[0] + 1u, tagB = ep[1] + 1u;
  auto ld_row = [&](auto R) __attribute__((always_inline)) {
    constexpr int i = R;
    if constexpr (i < 4) { const unsigned so = (fcol + 4 * w + i) * 1280; ra[i] = LD128(rWd, lane * 16, so, W_AUX); rb[i] = LD32(rWd, 1024 + lane * 4, so, W_AUX); }
    else { const unsigned so = (h * 128 + 16 * w + (i - 4)) * 1280; ra[i] = LD128(rWo, lane * 16, so, W_AUX); rb[i] = LD32(rWo, 1024 + lane * 4, so, W_AUX); }
    SB();
  };
  sfor<32>([&](auto T) __attribute__((always_inline)) {
    constexpr int t = T;
    constexpr int nx = icum(t) > icum(t - 1) ? icum(t - 1) : 32;
    if constexpr (t == 16) {
      STAMP(4);
      CB();
      const f32x4 s4 = *(const f32x4*)&sm.sc[w][lane * 4];
      mw = wave_max(fmaxf(fmaxf(s4[0], s4[1]), fmaxf(s4[2], s4[3])));
      const float p0 = exp_(s4[0] - mw), p1 = exp_(s4[1] - mw), p2 = exp_(s4[2] - mw), p3 = exp_(s4[3] - mw);
      const unsigned q01 = pk(p0, p1), q23 = pk(p2, p3);
      u32x4 pw = {q01 & 0xffffu, q01 >> 16, q23 & 0xffffu, q23 >> 16};
      *(u32x4*)&sm.sc[w][lane * 4] = pw;
      const float lw = wave_sum((p0 + p1) + (p2 + p3));
      u32x4 g = {f2u(mw), tagA, f2u(lw), tagB};
      __builtin_amdgcn_raw_buffer_store_b128(g, rWs, lane == 0 ? 0u : 0x80000000u, WS_ML + (b * H + h) * 256 + (half * 8 + w) * 16, SC1);
      CB();
      SB();
    }
    if constexpr (t == 31) {
      if (__builtin_expect(!__all(ml[1] == tagA && ml[3] == tagB), 0)) {
        unsigned spins = 0;
        do {
          __builtin_amdgcn_s_sleep(2);
          CB();
          ml = LD128(rWs, c * 16, WS_ML + (b * H + h) * 256, SC1);
        } while (!__all(ml[1] == tagA && ml[3] == tagB) && ++spins < SPIN_CAP);
      }
      const float mi = u2f(ml[0]), M = row_max(mi);
      const float L = row_sum(u2f(ml[2]) * exp_(mi - M));
      fw = exp_(mw - M) / L;
      asm volatile("" : "+v"(fw));
      SB();
    }
    if constexpr (t < 16) {
      float a[4];
      sfor<4>([&](auto J) __attribute__((always_inline)) {
        constexpr int j = J;
        float u = dot2(kv[t][j][0], qp[0], 0.f); u = dot2(kv[t][j][1], qp[1], u); u = dot2(kv[t][j][2], qp[2], u); a[j] = dot2(kv[t][j][3], qp[3], u);
        SB();
        ld_kv1(IC<nx>{}, J);
      });
      const float u0 = (b3 ? a[1] : a[0]) + dpp<ROR8>(b3 ? a[0] : a[1]);
      const float u1 = (b3 ? a[3] : a[2]) + dpp<ROR8>(b3 ? a[2] : a[3]);
      float v = (b2 ? u1 : u0) + dpp<HMIR>(b2 ? u0 : u1);
      v += dpp<XOR1>(v); v += dpp<XOR2>(v);
      scw[t * 16] = rbf(rbf(v) * 0.08838834764831845f);
    } else {
      const u32x4 pv = *(const u32x4*)(pr + (t - 16) * 16);
      sfor<4>([&](auto J) __attribute__((always_inline)) {
        constexpr int j = J;
        const unsigned pl = pv[j], ph = pl << 16;
#pragma unroll
        for (int k = 0; k < 4; ++k) { acc[2 * k] = dot2(kv[t][j][k], pl, acc[2 * k]); acc[2 * k + 1] = dot2(kv[t][j][k], ph, acc[2 * k + 1]); }
        SB();
        ld_kv1(IC<nx>{}, J);
      });
#pragma unroll
      for (int k = 0; k < 8; ++k) asm volatile("" : "+v"(acc[k]));
    }
    SB();
    if constexpr (t == PK - 1) STAMP(3);
    if constexpr (icum(t) > icum(t - 1) + 1) sfor<icum(t) - icum(t - 1) - 1>([&](auto I) __attribute__((always_inline)) { ld_kv(IC<icum(t - 1) + 1 + I>{}); });
    sfor<xcum(t + 1) - xcum(t)>([&](auto I) __attribute__((always_inline)) {
      constexpr int i = xcum(t) + I;
      if constexpr (i >= 10) ld_row(IC<i - 10>{});
      else if constexpr (i & 1) { uv[i / 2] = LD128(rWu, gu_vo, gu_so + (i / 2) * 3456, W_AUX); SB(); }
      else { gv[i / 2] = LD128(rWg, gu_vo, gu_so + (i / 2) * 3456, W_AUX); SB(); }
    });
    if constexpr (t == T_ML) { ml = LD128(rWs, c * 16, WS_ML + (b * H + h) * 256, SC1); SB(); }
    if constexpr (t == T_FF) {
      const unsigned short* xs = (const unsigned short*)&sm.xn[w][0] + 80 * w + 5 * (lane >> 2);
      float a[16];
#pragma unroll
      for (int k = 0; k < 16; ++k) a[k] = 0.f;
#pragma unroll
      for (int i = 0; i < 5; ++i) {
        const unsigned xl = xs[i], xh = xl << 16;
#pragma unroll
        for (int k = 0; k < 4; ++k) {
          a[2 * k] = dot2(gv[i][k], xl, a[2 * k]); a[2 * k + 1] = dot2(gv[i][k], xh, a[2 * k + 1]);
          a[8 + 2 * k] = dot2(uv[i][k], xl, a[8 + 2 * k]); a[9 + 2 * k] = dot2(uv[i][k], xh, a[9 + 2 * k]);
        }
      }
      float e[8];
#pragma unroll
      for (int k = 0; k < 8; ++k) e[k] = swap32_add(a[k], a[k + 8]);
      f32x4 f;
#pragma unroll
      for (int k = 0; k < 4; ++k) { float u = swap16_add(e[k], e[k + 4]); u += dpp<ROR4>(u); u += dpp<ROR8>(u); f[k] = u; }
      *(f32x4*)&sm.fpart[w][(lane >> 5) * 32 + (lane & 3) * 8 + ((lane >> 4) & 1) * 4] = f;
      CB();
      if (lane == 0) __hip_atomic_fetch_add(&sm.cnt, 1u, __ATOMIC_RELAXED, __HIP_MEMORY_SCOPE_WORKGROUP);
      CB();
      SB();
    }
    if constexpr (t == T_H) {
      unsigned spins = 0;
      while (__builtin_amdgcn_readfirstlane(__hip_atomic_load(&sm.cnt, __ATOMIC_RELAXED, __HIP_MEMORY_SCOPE_WORKGROUP)) != 8u && ++spins < SPIN_CAP)
        __builtin_amdgcn_s_sleep(1);
      CB();
      const float fsum = sm.fpart[lane >> 3][((lane >> 2) & 1) * 32 + 4 * w + (lane & 3)];
      float v = fsum + dpp<ROR8>(fsum);
      v = rbf(xrow_sum(v));
      const float gs = rbf(v / (1.f + exp_(-v)));
      const float hh = gs * dpp<SHL4>(v);
      const unsigned hp = pk(hh, dpp<XOR1>(hh));
      const unsigned h01 = __builtin_amdgcn_readlane(hp, 0), h23 = __builtin_amdgcn_readlane(hp, 2);
#pragma unroll
      for (int k = 0; k < 4; ++k) {
        y[2 * k] = dot2(plo(ra[2][k], ra[3][k]), h23, dot2(plo(ra[0][k], ra[1][k]), h01, 0.f));
        y[2 * k + 1] = dot2(phi(ra[2][k], ra[3][k]), h23, dot2(phi(ra[0][k], ra[1][k]), h01, 0.f));
      }
      y[8] = dot2(plo(rb[2], rb[3]), h23, dot2(plo(rb[0], rb[1]), h01, 0.f));
      y[9] = dot2(phi(rb[2], rb[3]), h23, dot2(phi(rb[0], rb[1]), h01, 0.f));
#pragma unroll
      for (int k = 0; k < 10; ++k) asm volatile("" : "+v"(y[k]));
      SB();
    }
    sfor<pcum(t + 1) - pcum(t)>([&](auto I) __attribute__((always_inline)) {
      constexpr int i = pcum(t) + I;
#pragma unroll
      for (int k = 0; k < 4; ++k) { wp[i][2 * k] = plo(ra[4 + 2 * i][k], ra[5 + 2 * i][k]); wp[i][2 * k + 1] = phi(ra[4 + 2 * i][k], ra[5 + 2 * i][k]); }
      wp[i][8] = plo(rb[4 + 2 * i], rb[5 + 2 * i]); wp[i][9] = phi(rb[4 + 2 * i], rb[5 + 2 * i]);
#pragma unroll
      for (int k = 0; k < 10; ++k) asm volatile("" : "+v"(wp[i][k]));
      SB();
    });
  });
  STAMP(5);
  {
    float e[4];
#pragma unroll
    for (int k = 0; k < 4; ++k) e[k] = swap32_add(acc[k], acc[k + 4]);
    f32x2 g = {swap16_add(e[0], e[2]) * fw, swap16_add(e[1], e[3]) * fw};
    *(f32x2*)&sm.opart[w][8 * c + 2 * r] = g;
  }
  BARRIER();
  {
    float v = sm.opart[2 * r][16 * w + c] + sm.opart[2 * r + 1][16 * w + c];
    v = xrow_sum(v);
    const unsigned op = pk(v, dpp<XOR1>(v));
#pragma unroll
    for (int i = 0; i < 8; ++i) {
      const unsigned oi = __builtin_amdgcn_readlane(op, 2 * i);
#pragma unroll
      for (int k = 0; k < 10; ++k) y[k] = dot2(wp[i][k], oi, y[k]);
    }
    f32x4 y0 = {y[0], y[1], y[2], y[3]}, y1 = {y[4], y[5], y[6], y[7]};
    f32x2 y2 = {y[8], y[9]};
    *(f32x4*)&sm.ypart[w][lane * 4] = y0;
    *(f32x4*)&sm.ypart[w][256 + lane * 4] = y1;
    *(f32x2*)&sm.ypart[w][512 + lane * 2] = y2;
  }
  BARRIER();
  if (w < 5) {
    f32x2 s = *(const f32x2*)&sm.ypart[0][2 * tid];
#pragma unroll
    for (int i = 1; i < 8; ++i) s += *(const f32x2*)&sm.ypart[i][2 * tid];
    u32x4 g = {f2u(s[0]), tagA, f2u(s[1]), tagB};
    __builtin_amdgcn_raw_buffer_store_b128(g, rWs, tid * 16, WS_Y + (b * 64 + h * 2 + half) * 5120, SC1);
  }
  STAMP(6);
  if (!is_red) return;
  {
    u32x4 g[8];
    const unsigned so = WS_Y + (b * 64 + w * 8) * 5120 + rj * 1024;
#pragma unroll
    for (int i = 0; i < 8; ++i) g[i] = LD128(rWs, lane * 16, so + i * 5120, SC1);
    unsigned spins = 0;
    for (;;) {
      bool all = true;
      CB();
#pragma unroll
      for (int i = 0; i < 8; ++i) {
        if (!__all(g[i][1] == tagA && g[i][3] == tagB)) { all = false; g[i] = LD128(rWs, lane * 16, so + i * 5120, SC1); }
      }
      if (all || ++spins > SPIN_CAP) break;
      __builtin_amdgcn_s_sleep(1);
    }
    f32x2 s = {0.f, 0.f};
#pragma unroll
    for (int i = 0; i < 8; ++i) { s[0] += u2f(g[i][0]); s[1] += u2f(g[i][2]); }
    *(f32x2*)&sm.red[w][2 * lane] = s;
  }
  BARRIER();
  if (w == 0) {
    f32x2 s = *(const f32x2*)&sm.red[0][2 * lane];
#pragma unroll
    for (int i = 1; i < 8; ++i) s += *(const f32x2*)&sm.red[i][2 * lane];
    *(unsigned*)(out + b * D + ocol) = pk(blo(xo) + s[0], bhi(xo) + s[1]);
    if (rj == 0 && lane == 0) {
      u32x2 e = {tagA, tagB};
      __builtin_amdgcn_raw_buffer_store_b64(e, rWs, 0, WS_EPOCH + b * 256, SC1);
    }
  }
  STAMP(7);
}

extern "C" void kernel_launch(void* const* d_in, const int* in_sizes, int n_in, void* d_out, int out_size, void* d_ws, size_t ws_size,
                              hipStream_t stream) {
  fused_decode<<<256, 512, 0, stream>>>((const unsigned short*)d_in[0], (const unsigned short*)d_in[1], (const unsigned short*)d_in[2],
                                        (const unsigned short*)d_in[3], (const unsigned short*)d_in[4], (const unsigned short*)d_in[6],
                                        (const unsigned short*)d_in[7], (const unsigned short*)d_in[8], (unsigned short*)d_out,
                                        (unsigned char*)d_ws);
}
```

```cpp
#include <hip/hip_runtime.h>
#include <hip/hip_bf16.h>
#include <stdint.h>
#include <utility>

typedef unsigned u32x4 __attribute__((ext_vector_type(4)));
typedef unsigned u32x2 __attribute__((ext_vector_type(2)));
typedef float f32x4 __attribute__((ext_vector_type(4)));
typedef float f32x2 __attribute__((ext_vector_type(2)));
typedef __bf16 bf16x2 __attribute__((ext_vector_type(2)));

#define W_AUX 0
#define WQ_AUX 2
#define WO_AUX W_AUX
#define FF_AUX 2
#define K_AUX 2
#define V_AUX 2
constexpr int SC1 = 16;
constexpr int D = 640, H = 32, F = 1728;
constexpr unsigned WS_EPOCH = 0, WS_ML = 4096, WS_Y = 65536, WS_BYTES = WS_Y + 4 * 64 * 5120;
constexpr unsigned SPIN_CAP = 1u << 18;

#define LD128(r, vo, so, aux) __builtin_amdgcn_raw_buffer_load_b128(r, vo, so, aux)
#define LD64(r, vo, so, aux) __builtin_amdgcn_raw_buffer_load_b64(r, vo, so, aux)
#define LD32(r, vo, so, aux) __builtin_amdgcn_raw_buffer_load_b32(r, vo, so, aux)
#define SB() __builtin_amdgcn_sched_barrier(0)
#define ST128(v, r, vo, so) do { __builtin_amdgcn_raw_buffer_store_b128(v, r, vo, so, SC1); SB(); asm volatile("s_nop 1"); SB(); } while (0)
#define RING 1
#define RSB() __builtin_amdgcn_sched_barrier(0)
#define CB() asm volatile("" ::: "memory")
#define BARRIER() asm volatile("s_waitcnt lgkmcnt(0)\n\ts_barrier" ::: "memory")

__device__ __forceinline__ float u2f(unsigned u) { return __builtin_bit_cast(float, u); }
__device__ __forceinline__ unsigned f2u(float f) { return __builtin_bit_cast(unsigned, f); }
__device__ __forceinline__ float blo(unsigned u) { return u2f(u << 16); }
__device__ __forceinline__ float bhi(unsigned u) { return u2f(u & 0xffff0000u); }
__device__ __forceinline__ unsigned pk(float lo, float hi) {
  f32x2 v = {lo, hi};
  return __builtin_bit_cast(unsigned, __builtin_convertvector(v, bf16x2));
}
__device__ __forceinline__ float rbf(float f) { return blo(pk(f, f)); }
__device__ __forceinline__ float dot2(unsigned a, unsigned b, float acc) {
  return __builtin_amdgcn_fdot2_f32_bf16(__builtin_bit_cast(bf16x2, a), __builtin_bit_cast(bf16x2, b), acc, false);
}
template <int CTRL> __device__ __forceinline__ float dpp(float x) {
  return u2f((unsigned)__builtin_amdgcn_mov_dpp((int)f2u(x), CTRL, 0xf, 0xf, true));
}
constexpr int XOR1 = 0xB1, XOR2 = 0x4E, HMIR = 0x141, ROR8 = 0x128, ROR4 = 0x124, SHL4 = 0x104;
__device__ __forceinline__ float swap32_add(float a, float b) {
  auto t = __builtin_amdgcn_permlane32_swap(f2u(a), f2u(b), false, false);
  return u2f(t[0]) + u2f(t[1]);
}
__device__ __forceinline__ float swap16_add(float a, float b) {
  auto t = __builtin_amdgcn_permlane16_swap(f2u(a), f2u(b), false, false);
  return u2f(t[0]) + u2f(t[1]);
}
__device__ __forceinline__ float xrow_sum(float x) { x = swap16_add(x, x); return swap32_add(x, x); }
__device__ __forceinline__ float xrow_max(float x) {
  auto s = __builtin_amdgcn_permlane16_swap(f2u(x), f2u(x), false, false);
  x = fmaxf(u2f(s[0]), u2f(s[1]));
  auto t = __builtin_amdgcn_permlane32_swap(f2u(x), f2u(x), false, false);
  return fmaxf(u2f(t[0]), u2f(t[1]));
}
__device__ __forceinline__ float row_sum(float v) { v += dpp<XOR1>(v); v += dpp<XOR2>(v); v += dpp<HMIR>(v); v += dpp<ROR8>(v); return v; }
__device__ __forceinline__ float row_max(float v) {
  v = fmaxf(v, dpp<XOR1>(v)); v = fmaxf(v, dpp<XOR2>(v)); v = fmaxf(v, dpp<HMIR>(v)); v = fmaxf(v, dpp<ROR8>(v)); return v;
}
__device__ __forceinline__ float wave_sum(float v) { return xrow_sum(row_sum(v)); }
__device__ __forceinline__ float wave_max(float v) { return xrow_max(row_max(v)); }
__device__ __forceinline__ unsigned plo(unsigned a, unsigned b) { return __builtin_amdgcn_perm(b, a, 0x05040100u); }
__device__ __forceinline__ unsigned phi(unsigned a, unsigned b) { return __builtin_amdgcn_perm(b, a, 0x07060302u); }
__device__ __forceinline__ float exp_(float x) { return __builtin_amdgcn_exp2f(x * 1.4426950408889634f); }

#define STAMP(i) do { } while (0)

template <int N> struct IC { static constexpr int value = N; constexpr operator int() const { return N; } };
template <typename Fn, int... I> __device__ __forceinline__ void sfor_impl(Fn&& f, std::integer_sequence<int, I...>) { (f(IC<I>{}), ...); }
template <int N, typename Fn> __device__ __forceinline__ void sfor(Fn&& f) { sfor_impl(f, std::make_integer_sequence<int, N>{}); }
#define ROT 1
#define EARLY 1
#define EPS 1
#define XOL 1
#define CPF 0
#define HMAP 1
#define PSEQ "wwwKwwwKwwwKwwwKwwwKwwwKwwK"
#define WN 8
#define XB 2
#define T_D2 32
#define T_D3 32
constexpr char pseq[] = PSEQ;
constexpr int PLEN = sizeof(pseq) - 1;
constexpr int pcount(int n, char ch) { int a = 0; for (int i = 0; i < n; ++i) a += pseq[i] == ch; return a; }
constexpr int PK = pcount(PLEN, 'K');
constexpr int PNL = 20 + 4 * PK;
static_assert(pcount(PLEN, 'w') == 20, "20 wq loads");
constexpr int pload(int n) { int k = 0; for (int i = 0; i < PLEN; ++i) { const int len = pseq[i] == 'w' ? 1 : 4; if (n < k + len) return pseq[i] == 'w' ? pcount(i, 'w') : -(1 + 4 * pcount(i, 'K') + (n - k)); k += len; } return 0; }
constexpr int wq_done(int n) { int a = 0; for (int i = 0; i < n && i < PNL; ++i) a += pload(i) >= 0; return a / 2; }
constexpr int icum(int t) {
  int v = PK + (XB >= 1 ? 1 : 0);
  if (XB >= 2 && t >= PK / 2) v = PK + 2;
  if (XB >= 3 && t >= PK - 1) v = PK + 3;
  const int d = t >= T_D3 ? 3 : (t >= T_D2 ? 2 : 1);
  if (t + 1 + d > v) v = t + 1 + d;
  return v > 32 ? 32 : v;
}
#define T_X0 (PK + XB - 1)
#define T_X1 29
#define GUW 8
constexpr int xstep(int i) { const int cw = i < 10 ? GUW * i : GUW * 10 + 5 * (i - 10); return T_X0 + cw * (T_X1 - T_X0) / (GUW * 10 + 100); }
constexpr int xcum(int t) { int a = 0; for (int i = 0; i < 30; ++i) a += xstep(i) < t; return a; }
constexpr int pcum(int t) { const int n = xcum(t - 2) - 14; return n < 0 ? 0 : n / 2; }
constexpr int T_FF = xstep(9) + 2;
constexpr int T_H = xstep(13) + 2 > T_FF + 3 ? xstep(13) + 2 : T_FF + 3;
constexpr int T_ML = 28;
static_assert(pcum(32) == 8 && T_H < 31, "schedule must finish inside the stream");
constexpr int vmcnt_imm(int n) { return (n & 15) | ((n >> 4) << 14) | 0x0F70; }

struct Smem {
  unsigned xn[8][320];
  float qpart[8][128];
  float fpart[8][64];
  unsigned qv[8][64];
  float sc[8][256];
  float opart[8][128];
  float ypart[8][640];
  float red[8][128];
  unsigned cnt;
};

__global__ void __launch_bounds__(512, 2)
fused_decode(const unsigned short* __restrict__ x, const unsigned short* __restrict__ kc, const unsigned short* __restrict__ vc,
             const unsigned short* __restrict__ wq, const unsigned short* __restrict__ wo, const unsigned short* __restrict__ wg,
             const unsigned short* __restrict__ wu, const unsigned short* __restrict__ wd, unsigned short* __restrict__ out,
             unsigned char* ws) {
  __shared__ __attribute__((aligned(16))) Smem sm;
  const int tid = threadIdx.x, lane = tid & 63;
  const int w = __builtin_amdgcn_readfirstlane(tid >> 6);
  const int bid = blockIdx.x, x8 = bid & 7, rr_ = bid >> 3, half = rr_ & 1, b = (rr_ >> 1) & 3, hhi = rr_ >> 3, h = HMAP ? x8 * 4 + hhi : hhi * 8 + x8;
  const int r = lane >> 4, c = lane & 15;
  STAMP(0);
  asm volatile("" :: "s"(x), "s"(kc), "s"(vc), "s"(wq), "s"(wo), "s"(wg), "s"(wu), "s"(wd), "s"(out), "s"(ws));

  const unsigned region = (unsigned)((b * H + h) * 2 + half);
  auto rX = __builtin_amdgcn_make_buffer_rsrc((void*)x, 0, 4 * D * 2, 0x00020000);
  auto rK = __builtin_amdgcn_make_buffer_rsrc((void*)(kc + (size_t)region * (2048 * 128)), 0, 512 * 1024, 0x00020000);
  auto rV = __builtin_amdgcn_make_buffer_rsrc((void*)(vc + (size_t)region * (2048 * 128)), 0, 512 * 1024, 0x00020000);
  auto rWq = __builtin_amdgcn_make_buffer_rsrc((void*)wq, 0, D * 4096 * 2, 0x00020000);
  auto rWo = __builtin_amdgcn_make_buffer_rsrc((void*)wo, 0, D * 4096 * 2, 0x00020000);
  auto rWs = __builtin_amdgcn_make_buffer_rsrc((void*)ws, 0, WS_BYTES, 0x00020000);
  const int slot = hhi * 2 + half, fcol = (x8 < 3 ? 4 * x8 : 3 * x8 + 3) * 64 + slot * 32;
  const bool has_ffn = slot < (x8 < 3 ? 8 : 6);
  auto rWg = __builtin_amdgcn_make_buffer_rsrc((void*)wg, 0, has_ffn ? D * F * 2 : 0, 0x00020000);
  auto rWu = __builtin_amdgcn_make_buffer_rsrc((void*)wu, 0, has_ffn ? D * F * 2 : 0, 0x00020000);
  auto rWd = __builtin_amdgcn_make_buffer_rsrc((void*)wd, 0, has_ffn ? F * D * 2 : 0, 0x00020000);
  const int rj = (h - b * 5) & 31;
  const bool is_red = (half == 0) && (rj < 5);
  const int gp = rj * 64 + lane;
  const int ocol = gp < 128 ? 8 * (gp >> 1) + 2 * (gp & 1) : (gp < 256 ? 8 * ((gp - 128) >> 1) + 4 + 2 * (gp & 1) : 512 + 2 * (gp - 256));

  u32x4 cpf;
  if constexpr (CPF) {
    unsigned long long pc; unsigned len;
    asm volatile("s_getpc_b64 %0\n.Lpf_begin:\n\ts_mov_b32 %1, .Lpf_end-.Lpf_begin" : "=s"(pc), "=s"(len));
    auto rC = __builtin_amdgcn_make_buffer_rsrc((void*)(pc & ~15ull), 0, w == 0 ? len : 0, 0x00020000);
    cpf = LD128(rC, lane * 16 + rr_ * 1024, 0, 0);
    SB();
  }
  const u32x4 xa = LD128(rX, lane * 16, b * 1280, 0);
  const unsigned xb = LD32(rX, 1024 + lane * 4, b * 1280, 0);
  const u32x2 ep = *(const __attribute__((address_space(4))) u32x2*)((unsigned long long)ws + WS_EPOCH + b * 256);
  if (tid == 0) sm.cnt = 0;
  SB();
  u32x4 wqv[20];
  u32x4 kv[33][4];
  const unsigned kvo = lane * 16;
  const int rot = ROT == 0 ? 0 : (ROT == 1 ? rr_ : (ROT == 2 ? 2 * x8 : rr_ + 2 * x8 + w));
  const unsigned wq_vo = r * (20 * 8192) + c * 16, wq_so = w * (80 * 8192) + h * 256;
  auto ld_kv1 = [&](auto T, auto J) __attribute__((always_inline)) {
    constexpr int t = T, j = J;
    if constexpr (t < 16) kv[t][j] = LD128(rK, kvo + j * 1024, (((t + rot) & 15) * 8 + w) * 4096, K_AUX);
    else if constexpr (t < 32) kv[t][j] = LD128(rV, kvo + j * 1024, (((t + rot) & 15) * 8 + w) * 4096, V_AUX);
    SB();
  };
  auto ld_kv = [&](auto T) __attribute__((always_inline)) { sfor<4>([&](auto J) __attribute__((always_inline)) { ld_kv1(T, J); }); };
  auto ld_pro = [&](auto N) __attribute__((always_inline)) {
    constexpr int p = pload(N);
    if constexpr (p >= 0) { wqv[p] = LD128(rWq, wq_vo, wq_so + p * 8192, WQ_AUX); SB(); }
    else ld_kv1(IC<(-p - 1) / 4>{}, IC<(-p - 1) % 4>{});
  };
  sfor<WN>([&](auto N) __attribute__((always_inline)) { ld_pro(N); });
  {
    float v[10] = {blo(xa[0]), bhi(xa[0]), blo(xa[1]), bhi(xa[1]), blo(xa[2]), bhi(xa[2]), blo(xa[3]), bhi(xa[3]), blo(xb), bhi(xb)};
    float s = 0.f;
#pragma unroll
    for (int i = 0; i < 10; ++i) s += v[i];
    const float mean = wave_sum(s) * (1.f / D);
    float s2 = 0.f;
#pragma unroll
    for (int i = 0; i < 10; ++i) { const float d = v[i] - mean; s2 += d * d; }
    const float var = wave_sum(s2) * (1.f / D);
    const float mb = rbf(mean), den = rbf(__builtin_sqrtf(rbf(rbf(var) + 1e-5f))), rd = 1.f / den;
#pragma unroll
    for (int i = 0; i < 10; ++i) v[i] = rbf(v[i] - mb) * rd;
    u32x4 o4 = {pk(v[0], v[1]), pk(v[2], v[3]), pk(v[4], v[5]), pk(v[6], v[7])};
    *(u32x4*)&sm.xn[w][lane * 4] = o4;
    sm.xn[w][256 + lane] = pk(v[8], v[9]);
  }
  CB();
  {
    unsigned xp[10];
    const unsigned* xs = &sm.xn[w][40 * w + 10 * r];
#pragma unroll
    for (int i = 0; i < 5; ++i) { const u32x2 t = *(const u32x2*)(xs + 2 * i); xp[2 * i] = t[0]; xp[2 * i + 1] = t[1]; }
    float a[8] = {0.f, 0.f, 0.f, 0.f, 0.f, 0.f, 0.f, 0.f};
    SB();
    auto qpair = [&](auto I) __attribute__((always_inline)) {
      constexpr int i = I;
#pragma unroll
      for (int k = 0; k < 4; ++k) {
        a[2 * k] = dot2(plo(wqv[2 * i][k], wqv[2 * i + 1][k]), xp[i], a[2 * k]);
        a[2 * k + 1] = dot2(phi(wqv[2 * i][k], wqv[2 * i + 1][k]), xp[i], a[2 * k + 1]);
      }
      SB();
    };
    sfor<PNL - WN>([&](auto I) __attribute__((always_inline)) {
      constexpr int n = I + WN;
      __builtin_amdgcn_s_waitcnt(vmcnt_imm(WN - 1));
      SB();
      ld_pro(IC<n>{});
      sfor<wq_done(n - WN + 1) - wq_done(n - WN)>([&](auto J) __attribute__((always_inline)) { qpair(IC<wq_done(n - WN) + J>{}); });
    });
    sfor<10 - wq_done(PNL - WN)>([&](auto J) __attribute__((always_inline)) { qpair(IC<wq_done(PNL - WN) + J>{}); });
    float e[4];
#pragma unroll
    for (int k = 0; k < 4; ++k) e[k] = swap32_add(a[k], a[k + 4]);
    f32x2 g = {swap16_add(e[0], e[2]), swap16_add(e[1], e[3])};
    *(f32x2*)&sm.qpart[w][8 * c + 2 * r] = g;
  }
  if constexpr (CPF) asm volatile("" :: "v"(cpf));
  STAMP(1);
  BARRIER();
  sfor<icum(-1) - PK>([&](auto I) __attribute__((always_inline)) { ld_kv(IC<PK + I>{}); });
  u32x4 qp;
  {
    f32x2 s = *(const f32x2*)&sm.qpart[0][2 * lane];
#pragma unroll
    for (int i = 1; i < 8; ++i) s += *(const f32x2*)&sm.qpart[i][2 * lane];
    sm.qv[w][lane] = pk(s[0], s[1]);
    CB();
    qp = *(const u32x4*)&sm.qv[w][4 * c];
  }
  STAMP(2);

  const bool b3 = lane & 8, b2 = lane & 4;
  float* scw = &sm.sc[w][r * 4 + ((lane >> 3) & 1) + ((lane >> 1) & 2)];
  const unsigned* pr = (const unsigned*)&sm.sc[w][r * 4];
  const unsigned gu_vo = (lane >> 2) * (5 * 3456) + (lane & 3) * 16, gu_so = w * (80 * 3456) + fcol * 2;
  u32x4 gv[5], uv[5];
  u32x4 ra[20]; unsigned rb[20];
  unsigned wp[8][10];
  float y[10];
  float acc[8] = {0.f, 0.f, 0.f, 0.f, 0.f, 0.f, 0.f, 0.f};
  float mw = 0.f, fw = 0.f;
  u32x4 ml;
  unsigned tagA = ep[0] + 1u, tagB = ep[1] + 1u;
  asm volatile("" : "+s"(tagA), "+s"(tagB));
  auto ld_row = [&](auto R) __attribute__((always_inline)) {
    constexpr int i = R;
    if constexpr (i < 4) { const unsigned so = (fcol + 4 * w + i) * 1280; ra[i] = LD128(rWd, lane * 16, so, FF_AUX); rb[i] = LD32(rWd, 1024 + lane * 4, so, FF_AUX); }
    else { const unsigned so = (h * 128 + 16 * w + (i - 4)) * 1280; ra[i] = LD128(rWo, lane * 16, so, WO_AUX); rb[i] = LD32(rWo, 1024 + lane * 4, so, WO_AUX); }
    SB();
  };
  sfor<32>([&](auto T) __attribute__((always_inline)) {
    constexpr int t = T;
    constexpr int nx = icum(t) > icum(t - 1) ? icum(t - 1) : 32;
    if constexpr (t == 16) {
      STAMP(4);
      CB();
      const f32x4 s4 = *(const f32x4*)&sm.sc[w][lane * 4];
      mw = wave_max(fmaxf(fmaxf(s4[0], s4[1]), fmaxf(s4[2], s4[3])));
      const float p0 = exp_(s4[0] - mw), p1 = exp_(s4[1] - mw), p2 = exp_(s4[2] - mw), p3 = exp_(s4[3] - mw);
      const unsigned q01 = pk(p0, p1), q23 = pk(p2, p3);
      u32x4 pw = {q01 & 0xffffu, q01 >> 16, q23 & 0xffffu, q23 >> 16};
      *(u32x4*)&sm.sc[w][lane * 4] = pw;
      const float lw = wave_sum((p0 + p1) + (p2 + p3));
      u32x4 g = {f2u(mw), tagA, f2u(lw), tagB};
      ST128(g, rWs, lane == 0 ? 0u : 0x80000000u, WS_ML + (b * H + h) * 256 + (half * 8 + w) * 16);
      CB();
      SB();
    }
    if constexpr (t == 31) {
      if (__builtin_expect(!__all(ml[1] == tagA && ml[3] == tagB), 0)) {
        unsigned spins = 0;
        do {
          __builtin_amdgcn_s_sleep(2);
          CB();
          ml = LD128(rWs, c * 16, WS_ML + (b * H + h) * 256, SC1);
        } while (!__all(ml[1] == tagA && ml[3] == tagB) && ++spins < SPIN_CAP);
      }
      const float mi = u2f(ml[0]), M = row_max(mi);
      const float L = row_sum(u2f(ml[2]) * exp_(mi - M));
      fw = exp_(mw - M) / L;
      asm volatile("" : "+v"(fw));
      SB();
    }
    if constexpr (t < 16) {
      float a[4];
      sfor<4>([&](auto J) __attribute__((always_inline)) {
        constexpr int j = J;
        if constexpr (EARLY) { asm volatile("" :: "v"(kv[t][j])); SB(); ld_kv1(IC<nx>{}, J); }
        float u = dot2(kv[t][j][0], qp[0], 0.f); u = dot2(kv[t][j][1], qp[1], u); u = dot2(kv[t][j][2], qp[2], u); a[j] = dot2(kv[t][j][3], qp[3], u);
        SB();
        if constexpr (!EARLY) ld_kv1(IC<nx>{}, J);
      });
      const float u0 = (b3 ? a[1] : a[0]) + dpp<ROR8>(b3 ? a[0] : a[1]);
      const float u1 = (b3 ? a[3] : a[2]) + dpp<ROR8>(b3 ? a[2] : a[3]);
      float v = (b2 ? u1 : u0) + dpp<HMIR>(b2 ? u0 : u1);
      v += dpp<XOR1>(v); v += dpp<XOR2>(v);
      scw[t * 16] = rbf(rbf(v) * 0.08838834764831845f);
    } else {
      const u32x4 pv = *(const u32x4*)(pr + (t - 16) * 16);
      sfor<4>([&](auto J) __attribute__((always_inline)) {
        constexpr int j = J;
        if constexpr (EARLY) { asm volatile("" :: "v"(kv[t][j])); SB(); ld_kv1(IC<nx>{}, J); }
        const unsigned pl = pv[j], ph = pl << 16;
#pragma unroll
        for (int k = 0; k < 4; ++k) { acc[2 * k] = dot2(kv[t][j][k], pl, acc[2 * k]); acc[2 * k + 1] = dot2(kv[t][j][k], ph, acc[2 * k + 1]); }
        SB();
        if constexpr (!EARLY) ld_kv1(IC<nx>{}, J);
      });
#pragma unroll
      for (int k = 0; k < 8; ++k) asm volatile("" : "+v"(acc[k]));
    }
    SB();
    if constexpr (t == PK - 1) STAMP(3);
    if constexpr (icum(t) > icum(t - 1) + 1) sfor<icum(t) - icum(t - 1) - 1>([&](auto I) __attribute__((always_inline)) { ld_kv(IC<icum(t - 1) + 1 + I>{}); });
    sfor<xcum(t + 1) - xcum(t)>([&](auto I) __attribute__((always_inline)) {
      constexpr int i = xcum(t) + I;
      if constexpr (i >= 10) ld_row(IC<i - 10>{});
      else if constexpr (i & 1) { uv[i / 2] = LD128(rWu, gu_vo, gu_so + (i / 2) * 3456, FF_AUX); SB(); }
      else { gv[i / 2] = LD128(rWg, gu_vo, gu_so + (i / 2) * 3456, FF_AUX); SB(); }
    });
    if constexpr (t == T_ML) { ml = LD128(rWs, c * 16, WS_ML + (b * H + h) * 256, SC1); SB(); }
    if constexpr (t == T_FF) {
      const unsigned short* xs = (const unsigned short*)&sm.xn[w][0] + 80 * w + 5 * (lane >> 2);
      float a[16];
#pragma unroll
      for (int k = 0; k < 16; ++k) a[k] = 0.f;
#pragma unroll
      for (int i = 0; i < 5; ++i) {
        const unsigned xl = xs[i], xh = xl << 16;
#pragma unroll
        for (int k = 0; k < 4; ++k) {
          a[2 * k] = dot2(gv[i][k], xl, a[2 * k]); a[2 * k + 1] = dot2(gv[i][k], xh, a[2 * k + 1]);
          a[8 + 2 * k] = dot2(uv[i][k], xl, a[8 + 2 * k]); a[9 + 2 * k] = dot2(uv[i][k], xh, a[9 + 2 * k]);
        }
      }
      float e[8];
#pragma unroll
      for (int k = 0; k < 8; ++k) e[k] = swap32_add(a[k], a[k + 8]);
      f32x4 f;
#pragma unroll
      for (int k = 0; k < 4; ++k) { float u = swap16_add(e[k], e[k + 4]); u += dpp<ROR4>(u); u += dpp<ROR8>(u); f[k] = u; }
      *(f32x4*)&sm.fpart[w][(lane >> 5) * 32 + (lane & 3) * 8 + ((lane >> 4) & 1) * 4] = f;
      CB();
      if (lane == 0) __hip_atomic_fetch_add(&sm.cnt, 1u, __ATOMIC_RELAXED, __HIP_MEMORY_SCOPE_WORKGROUP);
      CB();
      SB();
    }
    if constexpr (t == T_H) {
      unsigned spins = 0;
      while (__builtin_amdgcn_readfirstlane(__hip_atomic_load(&sm.cnt, __ATOMIC_RELAXED, __HIP_MEMORY_SCOPE_WORKGROUP)) != 8u && ++spins < SPIN_CAP)
        __builtin_amdgcn_s_sleep(1);
      CB();
      const float fsum = sm.fpart[lane >> 3][((lane >> 2) & 1) * 32 + 4 * w + (lane & 3)];
      float v = fsum + dpp<ROR8>(fsum);
      v = rbf(xrow_sum(v));
      const float gs = rbf(v / (1.f + exp_(-v)));
      const float hh = gs * dpp<SHL4>(v);
      const unsigned hp = pk(hh, dpp<XOR1>(hh));
      const unsigned h01 = __builtin_amdgcn_readlane(hp, 0), h23 = __builtin_amdgcn_readlane(hp, 2);
#pragma unroll
      for (int k = 0; k < 4; ++k) {
        y[2 * k] = dot2(plo(ra[2][k], ra[3][k]), h23, dot2(plo(ra[0][k], ra[1][k]), h01, 0.f));
        y[2 * k + 1] = dot2(phi(ra[2][k], ra[3][k]), h23, dot2(phi(ra[0][k], ra[1][k]), h01, 0.f));
      }
      y[8] = dot2(plo(rb[2], rb[3]), h23, dot2(plo(rb[0], rb[1]), h01, 0.f));
      y[9] = dot2(phi(rb[2], rb[3]), h23, dot2(phi(rb[0], rb[1]), h01, 0.f));
#pragma unroll
      for (int k = 0; k < 10; ++k) asm volatile("" : "+v"(y[k]));
      SB();
    }
    sfor<pcum(t + 1) - pcum(t)>([&](auto I) __attribute__((always_inline)) {
      constexpr int i = pcum(t) + I;
#pragma unroll
      for (int k = 0; k < 4; ++k) { wp[i][2 * k] = plo(ra[4 + 2 * i][k], ra[5 + 2 * i][k]); wp[i][2 * k + 1] = phi(ra[4 + 2 * i][k], ra[5 + 2 * i][k]); }
      wp[i][8] = plo(rb[4 + 2 * i], rb[5 + 2 * i]); wp[i][9] = phi(rb[4 + 2 * i], rb[5 + 2 * i]);
#pragma unroll
      for (int k = 0; k < 10; ++k) asm volatile("" : "+v"(wp[i][k]));
      SB();
    });
  });
  STAMP(5);
  {
    float e[4];
#pragma unroll
    for (int k = 0; k < 4; ++k) e[k] = swap32_add(acc[k], acc[k + 4]);
    f32x2 g = {swap16_add(e[0], e[2]) * fw, swap16_add(e[1], e[3]) * fw};
    *(f32x2*)&sm.opart[w][8 * c + 2 * r] = g;
  }
  BARRIER();
  {
    float v = sm.opart[2 * r][16 * w + c] + sm.opart[2 * r + 1][16 * w + c];
    v = xrow_sum(v);
    const unsigned op = pk(v, dpp<XOR1>(v));
#pragma unroll
    for (int i = 0; i < 8; ++i) {
      const unsigned oi = __builtin_amdgcn_readlane(op, 2 * i);
#pragma unroll
      for (int k = 0; k < 10; ++k) y[k] = dot2(wp[i][k], oi, y[k]);
    }
    f32x4 y0 = {y[0], y[1], y[2], y[3]}, y1 = {y[4], y[5], y[6], y[7]};
    f32x2 y2 = {y[8], y[9]};
    *(f32x4*)&sm.ypart[w][lane * 4] = y0;
    *(f32x4*)&sm.ypart[w][256 + lane * 4] = y1;
    *(f32x2*)&sm.ypart[w][512 + lane * 2] = y2;
  }
  BARRIER();
  if (w < 5) {
    f32x2 s = *(const f32x2*)&sm.ypart[0][2 * tid];
#pragma unroll
    for (int i = 1; i < 8; ++i) s += *(const f32x2*)&sm.ypart[i][2 * tid];
    u32x4 g = {f2u(s[0]), tagA, f2u(s[1]), tagB};
    ST128(g, rWs, tid * 16, WS_Y + (b * 64 + h * 2 + half) * 5120);
  }
  STAMP(6);
  if (!is_red) return;
  const unsigned xo = LD32(rX, ocol * 2, b * 1280, 0);
  {
    u32x4 g[8];
    const unsigned so = WS_Y + (b * 64 + w * 8) * 5120 + rj * 1024;
#pragma unroll
    for (int i = 0; i < 8; ++i) g[i] = LD128(rWs, lane * 16, so + i * 5120, SC1);
    unsigned spins = 0;
    for (;;) {
      bool all = true;
      CB();
#pragma unroll
      for (int i = 0; i < 8; ++i) {
        if (!__all(g[i][1] == tagA && g[i][3] == tagB)) { all = false; g[i] = LD128(rWs, lane * 16, so + i * 5120, SC1); }
      }
      if (all || ++spins > SPIN_CAP) break;
      __builtin_amdgcn_s_sleep(1);
    }
    f32x2 s = {0.f, 0.f};
#pragma unroll
    for (int i = 0; i < 8; ++i) { s[0] += u2f(g[i][0]); s[1] += u2f(g[i][2]); }
    *(f32x2*)&sm.red[w][2 * lane] = s;
  }
  BARRIER();
  if (w == 0) {
    f32x2 s = *(const f32x2*)&sm.red[0][2 * lane];
#pragma unroll
    for (int i = 1; i < 8; ++i) s += *(const f32x2*)&sm.red[i][2 * lane];
    *(unsigned*)(out + b * D + ocol) = pk(blo(xo) + s[0], bhi(xo) + s[1]);
    if (rj == 0 && lane == 0) {
      u32x2 e = {tagA, tagB};
      __builtin_amdgcn_raw_buffer_store_b64(e, rWs, 0, WS_EPOCH + b * 256, SC1);
    }
  }
  STAMP(7);
  if constexpr (CPF) asm volatile(".Lpf_end:");
}

extern "C" void kernel_launch(void* const* d_in, const int* in_sizes, int n_in, void* d_out, int out_size, void* d_ws, size_t ws_size,
                              hipStream_t stream) {
  fused_decode<<<256, 512, 0, stream>>>((const unsigned short*)d_in[0], (const unsigned short*)d_in[1], (const unsigned short*)d_in[2],
                                        (const unsigned short*)d_in[3], (const unsigned short*)d_in[4], (const unsigned short*)d_in[6],
                                        (const unsigned short*)d_in[7], (const unsigned short*)d_in[8], (unsigned short*)d_out,
                                        (unsigned char*)d_ws);
}
```

```cpp
#include <hip/hip_runtime.h>
#include <hip/hip_bf16.h>
#include <stdint.h>
#include <utility>

typedef unsigned u32x4 __attribute__((ext_vector_type(4)));
typedef unsigned u32x2 __attribute__((ext_vector_type(2)));
typedef float f32x4 __attribute__((ext_vector_type(4)));
typedef float f32x2 __attribute__((ext_vector_type(2)));
typedef __bf16 bf16x2 __attribute__((ext_vector_type(2)));

#define W_AUX 0
#define WQ_AUX W_AUX
#define WO_AUX W_AUX
#define FF_AUX W_AUX
#define K_AUX 2
#define V_AUX 2
constexpr int SC1 = 16;
constexpr int D = 640, H = 32, F = 1728;
constexpr unsigned WS_EPOCH = 0, WS_ML = 4096, WS_Y = 65536, WS_BYTES = WS_Y + 4 * 64 * 5120;
constexpr unsigned SPIN_CAP = 1u << 18;

#define LD128(r, vo, so, aux) __builtin_amdgcn_raw_buffer_load_b128(r, vo, so, aux)
#define LD64(r, vo, so, aux) __builtin_amdgcn_raw_buffer_load_b64(r, vo, so, aux)
#define LD32(r, vo, so, aux) __builtin_amdgcn_raw_buffer_load_b32(r, vo, so, aux)
#define SB() __builtin_amdgcn_sched_barrier(0)
#define ST128(v, r, vo, so) do { __builtin_amdgcn_raw_buffer_store_b128(v, r, vo, so, SC1); SB(); asm volatile("s_nop 1"); SB(); } while (0)
#define RING 1
#define RSB() __builtin_amdgcn_sched_barrier(0)
#define CB() asm volatile("" ::: "memory")
#define BARRIER() asm volatile("s_waitcnt lgkmcnt(0)\n\ts_barrier" ::: "memory")

__device__ __forceinline__ float u2f(unsigned u) { return __builtin_bit_cast(float, u); }
__device__ __forceinline__ unsigned f2u(float f) { return __builtin_bit_cast(unsigned, f); }
__device__ __forceinline__ float blo(unsigned u) { return u2f(u << 16); }
__device__ __forceinline__ float bhi(unsigned u) { return u2f(u & 0xffff0000u); }
__device__ __forceinline__ unsigned pk(float lo, float hi) {
  f32x2 v = {lo, hi};
  return __builtin_bit_cast(unsigned, __builtin_convertvector(v, bf16x2));
}
__device__ __forceinline__ float rbf(float f) { return blo(pk(f, f)); }
__device__ __forceinline__ float dot2(unsigned a, unsigned b, float acc) {
  return __builtin_amdgcn_fdot2_f32_bf16(__builtin_bit_cast(bf16x2, a), __builtin_bit_cast(bf16x2, b), acc, false);
}
template <int CTRL> __device__ __forceinline__ float dpp(float x) {
  return u2f((unsigned)__builtin_amdgcn_mov_dpp((int)f2u(x), CTRL, 0xf, 0xf, true));
}
constexpr int XOR1 = 0xB1, XOR2 = 0x4E, HMIR = 0x141, ROR8 = 0x128, ROR4 = 0x124, SHL4 = 0x104;
__device__ __forceinline__ float swap32_add(float a, float b) {
  auto t = __builtin_amdgcn_permlane32_swap(f2u(a), f2u(b), false, false);
  return u2f(t[0]) + u2f(t[1]);
}
__device__ __forceinline__ float swap16_add(float a, float b) {
  auto t = __builtin_amdgcn_permlane16_swap(f2u(a), f2u(b), false, false);
  return u2f(t[0]) + u2f(t[1]);
}
__device__ __forceinline__ float xrow_sum(float x) { x = swap16_add(x, x); return swap32_add(x, x); }
__device__ __forceinline__ float xrow_max(float x) {
  auto s = __builtin_amdgcn_permlane16_swap(f2u(x), f2u(x), false, false);
  x = fmaxf(u2f(s[0]), u2f(s[1]));
  auto t = __builtin_amdgcn_permlane32_swap(f2u(x), f2u(x), false, false);
  return fmaxf(u2f(t[0]), u2f(t[1]));
}
__device__ __forceinline__ float row_sum(float v) { v += dpp<XOR1>(v); v += dpp<XOR2>(v); v += dpp<HMIR>(v); v += dpp<ROR8>(v); return v; }
__device__ __forceinline__ float row_max(float v) {
  v = fmaxf(v, dpp<XOR1>(v)); v = fmaxf(v, dpp<XOR2>(v)); v = fmaxf(v, dpp<HMIR>(v)); v = fmaxf(v, dpp<ROR8>(v)); return v;
}
__device__ __forceinline__ float wave_sum(float v) { return xrow_sum(row_sum(v)); }
__device__ __forceinline__ float wave_max(float v) { return xrow_max(row_max(v)); }
__device__ __forceinline__ unsigned plo(unsigned a, unsigned b) { return __builtin_amdgcn_perm(b, a, 0x05040100u); }
__device__ __forceinline__ unsigned phi(unsigned a, unsigned b) { return __builtin_amdgcn_perm(b, a, 0x07060302u); }
__device__ __forceinline__ float exp_(float x) { return __builtin_amdgcn_exp2f(x * 1.4426950408889634f); }

#define STAMP(i) do { } while (0)

template <int N> struct IC { static constexpr int value = N; constexpr operator int() const { return N; } };
template <typename Fn, int... I> __device__ __forceinline__ void sfor_impl(Fn&& f, std::integer_sequence<int, I...>) { (f(IC<I>{}), ...); }
template <int N, typename Fn> __device__ __forceinline__ void sfor(Fn&& f) { sfor_impl(f, std::make_integer_sequence<int, N>{}); }
#define ROT 1
#define EARLY 1
#define EPS 1
#define XOL 1
#define PT 7
#define PTP 0
#define CPF 0
#define HMAP 1
#define PSEQ "wwwKwwwKwwwKwwwKwwwKwwwKwwK"
#define WN 8
#define XB 2
#define T_D2 32
#define T_D3 32
constexpr char pseq[] = PSEQ;
constexpr int PLEN = sizeof(pseq) - 1;
constexpr int pcount(int n, char ch) { int a = 0; for (int i = 0; i < n; ++i) a += pseq[i] == ch; return a; }
constexpr int PK = pcount(PLEN, 'K');
constexpr int PNL = 20 + 4 * PK;
static_assert(pcount(PLEN, 'w') == 20, "20 wq loads");
constexpr int pload(int n) { int k = 0; for (int i = 0; i < PLEN; ++i) { const int len = pseq[i] == 'w' ? 1 : 4; if (n < k + len) return pseq[i] == 'w' ? pcount(i, 'w') : -(1 + 4 * pcount(i, 'K') + (n - k)); k += len; } return 0; }
constexpr int wq_done(int n) { int a = 0; for (int i = 0; i < n && i < PNL; ++i) a += pload(i) >= 0; return a / 2; }
constexpr int icum(int t) {
  int v = PK + (XB >= 1 ? 1 : 0);
  if (XB >= 2 && t >= PK / 2) v = PK + 2;
  if (XB >= 3 && t >= PK - 1) v = PK + 3;
  const int d = t >= T_D3 ? 3 : (t >= T_D2 ? 2 : 1);
  if (t + 1 + d > v) v = t + 1 + d;
  return v > 32 ? 32 : v;
}
#define T_X0 (PK + XB - 1)
#define T_X1 29
#define GUW 8
constexpr int xstep(int i) { const int cw = i < 10 ? GUW * i : GUW * 10 + 5 * (i - 10); return T_X0 + cw * (T_X1 - T_X0) / (GUW * 10 + 100); }
constexpr int xcum(int t) { int a = 0; for (int i = 0; i < 30; ++i) a += xstep(i) < t; return a; }
constexpr int pcum(int t) { const int n = xcum(t - 2) - 14; return n < 0 ? 0 : n / 2; }
constexpr int T_FF = xstep(9) + 2;
constexpr int T_H = xstep(13) + 2 > T_FF + 3 ? xstep(13) + 2 : T_FF + 3;
constexpr int T_ML = 28;
static_assert(pcum(32) == 8 && T_H < 31, "schedule must finish inside the stream");
constexpr int vmcnt_imm(int n) { return (n & 15) | ((n >> 4) << 14) | 0x0F70; }

struct Smem {
  unsigned xn[8][320];
  float qpart[8][128];
  float fpart[8][64];
  unsigned qv[8][64];
  float sc[8][256];
  float opart[8][128];
  float ypart[8][640];
  float red[8][128];
  unsigned cnt;
};

__global__ void __launch_bounds__(512, 2)
fused_decode(const unsigned short* __restrict__ x, const unsigned short* __restrict__ kc, const unsigned short* __restrict__ vc,
             const unsigned short* __restrict__ wq, const unsigned short* __restrict__ wo, const unsigned short* __restrict__ wg,
             const unsigned short* __restrict__ wu, const unsigned short* __restrict__ wd, unsigned short* __restrict__ out,
             unsigned char* ws) {
  __shared__ __attribute__((aligned(16))) Smem sm;
  const int tid = threadIdx.x, lane = tid & 63;
  const int w = __builtin_amdgcn_readfirstlane(tid >> 6);
  const int bid = blockIdx.x, x8 = bid & 7, rr_ = bid >> 3, half = rr_ & 1, b = (rr_ >> 1) & 3, hhi = rr_ >> 3, h = HMAP ? x8 * 4 + hhi : hhi * 8 + x8;
  const int r = lane >> 4, c = lane & 15;
  STAMP(0);
  asm volatile("" :: "s"(x), "s"(kc), "s"(vc), "s"(wq), "s"(wo), "s"(wg), "s"(wu), "s"(wd), "s"(out), "s"(ws));

  const unsigned region = (unsigned)((b * H + h) * 2 + half);
  auto rX = __builtin_amdgcn_make_buffer_rsrc((void*)x, 0, 4 * D * 2, 0x00020000);
  auto rK = __builtin_amdgcn_make_buffer_rsrc((void*)(kc + (size_t)region * (2048 * 128)), 0, 512 * 1024, 0x00020000);
  auto rV = __builtin_amdgcn_make_buffer_rsrc((void*)(vc + (size_t)region * (2048 * 128)), 0, 512 * 1024, 0x00020000);
  auto rWq = __builtin_amdgcn_make_buffer_rsrc((void*)wq, 0, D * 4096 * 2, 0x00020000);
  auto rWo = __builtin_amdgcn_make_buffer_rsrc((void*)wo, 0, D * 4096 * 2, 0x00020000);
  auto rWs = __builtin_amdgcn_make_buffer_rsrc((void*)ws, 0, WS_BYTES, 0x00020000);
  const int slot = hhi * 2 + half, fcol = (x8 < 3 ? 4 * x8 : 3 * x8 + 3) * 64 + slot * 32;
  const bool has_ffn = slot < (x8 < 3 ? 8 : 6);
  auto rWg = __builtin_amdgcn_make_buffer_rsrc((void*)wg, 0, has_ffn ? D * F * 2 : 0, 0x00020000);
  auto rWu = __builtin_amdgcn_make_buffer_rsrc((void*)wu, 0, has_ffn ? D * F * 2 : 0, 0x00020000);
  auto rWd = __builtin_amdgcn_make_buffer_rsrc((void*)wd, 0, has_ffn ? F * D * 2 : 0, 0x00020000);
  const int rj = (h - b * 5) & 31;
  const bool is_red = (half == 0) && (rj < 5);
  const int gp = rj * 64 + lane;
  const int ocol = gp < 128 ? 8 * (gp >> 1) + 2 * (gp & 1) : (gp < 256 ? 8 * ((gp - 128) >> 1) + 4 + 2 * (gp & 1) : 512 + 2 * (gp - 256));

  u32x4 cpf;
  if constexpr (CPF) {
    unsigned long long pc; unsigned len;
    asm volatile("s_getpc_b64 %0\n.Lpf_begin:\n\ts_mov_b32 %1, .Lpf_end-.Lpf_begin" : "=s"(pc), "=s"(len));
    auto rC = __builtin_amdgcn_make_buffer_rsrc((void*)(pc & ~15ull), 0, w == 0 ? len : 0, 0x00020000);
    cpf = LD128(rC, lane * 16 + rr_ * 1024, 0, 0);
    SB();
  }
  const u32x4 xa = LD128(rX, lane * 16, b * 1280, 0);
  const unsigned xb = LD32(rX, 1024 + lane * 4, b * 1280, 0);
  const u32x2 ep = *(const __attribute__((address_space(4))) u32x2*)((unsigned long long)ws + WS_EPOCH + b * 256);
  if (tid == 0) sm.cnt = 0;
  SB();
  unsigned pt[5];
  auto touch = [&](auto Q) __attribute__((always_inline)) {
    constexpr int q = Q;
    const unsigned wi = rr_ * 8 + w, nl = x8 < 3 ? 4 : 3, f0 = (x8 < 3 ? 4 * x8 : 3 * x8 + 3) * 64, off = 0x80000000u;
    if constexpr (q & 1) { pt[0] = LD32(rWo, lane < 40 ? lane * 64 : off, (x8 * 256 + wi) * 2560, WO_AUX); SB(); }
    if constexpr (q & 2) {
      auto rA = __builtin_amdgcn_make_buffer_rsrc((void*)wd, 0, F * D * 2, 0x00020000);
      pt[1] = LD32(rA, lane < 5 * nl ? lane * 64 : off, f0 * 1280 + wi * nl * 320, FF_AUX); SB();
    }
    if constexpr (q & 4) {
      auto rA = __builtin_amdgcn_make_buffer_rsrc((void*)wg, 0, F * D * 2, 0x00020000);
      auto rB = __builtin_amdgcn_make_buffer_rsrc((void*)wu, 0, F * D * 2, 0x00020000);
      const unsigned ch = wi * 20 + lane, vo = lane < 20 && (ch & 7) < 2 * nl ? (ch >> 3) * 3456 + (ch & 7) * 64 : off;
      pt[2] = LD32(rA, vo, f0 * 2, FF_AUX); SB();
      pt[3] = LD32(rB, vo, f0 * 2, FF_AUX); SB();
    }
    if constexpr (q & 8) {
      const unsigned ch = wi * 40 + lane;
      pt[4] = LD32(rWq, lane < 40 ? (ch >> 4) * 8192 + (ch & 15) * 64 : off, x8 * 1024, WQ_AUX); SB();
    }
  };
  auto untouch = [&](auto Q) __attribute__((always_inline)) {
    constexpr int q = Q;
    if constexpr (q & 1) asm volatile("" :: "v"(pt[0]));
    if constexpr (q & 2) asm volatile("" :: "v"(pt[1]));
    if constexpr (q & 4) asm volatile("" :: "v"(pt[2]), "v"(pt[3]));
    if constexpr (q & 8) asm volatile("" :: "v"(pt[4]));
  };
  static_assert(HMAP == 1 || PT == 0, "prefetch map");
  touch(IC<PT & 8>{});
  if constexpr (PTP == 0) touch(IC<PT & 7>{});
  u32x4 wqv[20];
  u32x4 kv[33][4];
  const unsigned kvo = lane * 16;
  const int rot = ROT == 0 ? 0 : (ROT == 1 ? rr_ : (ROT == 2 ? 2 * x8 : rr_ + 2 * x8 + w));
  const unsigned wq_vo = r * (20 * 8192) + c * 16, wq_so = w * (80 * 8192) + h * 256;
  auto ld_kv1 = [&](auto T, auto J) __attribute__((always_inline)) {
    constexpr int t = T, j = J;
    if constexpr (t < 16) kv[t][j] = LD128(rK, kvo + j * 1024, (((t + rot) & 15) * 8 + w) * 4096, K_AUX);
    else if constexpr (t < 32) kv[t][j] = LD128(rV, kvo + j * 1024, (((t + rot) & 15) * 8 + w) * 4096, V_AUX);
    SB();
  };
  auto ld_kv = [&](auto T) __attribute__((always_inline)) { sfor<4>([&](auto J) __attribute__((always_inline)) { ld_kv1(T, J); }); };
  auto ld_pro = [&](auto N) __attribute__((always_inline)) {
    constexpr int p = pload(N);
    if constexpr (p >= 0) { wqv[p] = LD128(rWq, wq_vo, wq_so + p * 8192, WQ_AUX); SB(); }
    else ld_kv1(IC<(-p - 1) / 4>{}, IC<(-p - 1) % 4>{});
  };
  sfor<WN>([&](auto N) __attribute__((always_inline)) { ld_pro(N); });
  {
    float v[10] = {blo(xa[0]), bhi(xa[0]), blo(xa[1]), bhi(xa[1]), blo(xa[2]), bhi(xa[2]), blo(xa[3]), bhi(xa[3]), blo(xb), bhi(xb)};
    float s = 0.f;
#pragma unroll
    for (int i = 0; i < 10; ++i) s += v[i];
    const float mean = wave_sum(s) * (1.f / D);
    float s2 = 0.f;
#pragma unroll
    for (int i = 0; i < 10; ++i) { const float d = v[i] - mean; s2 += d * d; }
    const float var = wave_sum(s2) * (1.f / D);
    const float mb = rbf(mean), den = rbf(__builtin_sqrtf(rbf(rbf(var) + 1e-5f))), rd = 1.f / den;
#pragma unroll
    for (int i = 0; i < 10; ++i) v[i] = rbf(v[i] - mb) * rd;
    u32x4 o4 = {pk(v[0], v[1]), pk(v[2], v[3]), pk(v[4], v[5]), pk(v[6], v[7])};
    *(u32x4*)&sm.xn[w][lane * 4] = o4;
    sm.xn[w][256 + lane] = pk(v[8], v[9]);
  }
  CB();
  {
    unsigned xp[10];
    const unsigned* xs = &sm.xn[w][40 * w + 10 * r];
#pragma unroll
    for (int i = 0; i < 5; ++i) { const u32x2 t = *(const u32x2*)(xs + 2 * i); xp[2 * i] = t[0]; xp[2 * i + 1] = t[1]; }
    float a[8] = {0.f, 0.f, 0.f, 0.f, 0.f, 0.f, 0.f, 0.f};
    SB();
    auto qpair = [&](auto I) __attribute__((always_inline)) {
      constexpr int i = I;
#pragma unroll
      for (int k = 0; k < 4; ++k) {
        a[2 * k] = dot2(plo(wqv[2 * i][k], wqv[2 * i + 1][k]), xp[i], a[2 * k]);
        a[2 * k + 1] = dot2(phi(wqv[2 * i][k], wqv[2 * i + 1][k]), xp[i], a[2 * k + 1]);
      }
      SB();
    };
    sfor<PNL - WN>([&](auto I) __attribute__((always_inline)) {
      constexpr int n = I + WN;
      __builtin_amdgcn_s_waitcnt(vmcnt_imm(WN - 1));
      SB();
      ld_pro(IC<n>{});
      sfor<wq_done(n - WN + 1) - wq_done(n - WN)>([&](auto J) __attribute__((always_inline)) { qpair(IC<wq_done(n - WN) + J>{}); });
    });
    sfor<10 - wq_done(PNL - WN)>([&](auto J) __attribute__((always_inline)) { qpair(IC<wq_done(PNL - WN) + J>{}); });
    float e[4];
#pragma unroll
    for (int k = 0; k < 4; ++k) e[k] = swap32_add(a[k], a[k + 4]);
    f32x2 g = {swap16_add(e[0], e[2]), swap16_add(e[1], e[3])};
    *(f32x2*)&sm.qpart[w][8 * c + 2 * r] = g;
  }
  if constexpr (CPF) asm volatile("" :: "v"(cpf));
  untouch(IC<PTP == 0 ? PT : (PT & 8)>{});
  STAMP(1);
  BARRIER();
  if constexpr (PTP == 1) touch(IC<PT & 7>{});
  sfor<icum(-1) - PK>([&](auto I) __attribute__((always_inline)) { ld_kv(IC<PK + I>{}); });
  u32x4 qp;
  {
    f32x2 s = *(const f32x2*)&sm.qpart[0][2 * lane];
#pragma unroll
    for (int i = 1; i < 8; ++i) s += *(const f32x2*)&sm.qpart[i][2 * lane];
    sm.qv[w][lane] = pk(s[0], s[1]);
    CB();
    qp = *(const u32x4*)&sm.qv[w][4 * c];
  }
  STAMP(2);

  const bool b3 = lane & 8, b2 = lane & 4;
  float* scw = &sm.sc[w][r * 4 + ((lane >> 3) & 1) + ((lane >> 1) & 2)];
  const unsigned* pr = (const unsigned*)&sm.sc[w][r * 4];
  const unsigned gu_vo = (lane >> 2) * (5 * 3456) + (lane & 3) * 16, gu_so = w * (80 * 3456) + fcol * 2;
  u32x4 gv[5], uv[5];
  u32x4 ra[20]; unsigned rb[20];
  unsigned wp[8][10];
  float y[10];
  float acc[8] = {0.f, 0.f, 0.f, 0.f, 0.f, 0.f, 0.f, 0.f};
  float mw = 0.f, fw = 0.f;
  u32x4 ml;
  unsigned tagA = ep[0] + 1u, tagB = ep[1] + 1u;
  asm volatile("" : "+s"(tagA), "+s"(tagB));
  auto ld_row = [&](auto R) __attribute__((always_inline)) {
    constexpr int i = R;
    if constexpr (i < 4) { const unsigned so = (fcol + 4 * w + i) * 1280; ra[i] = LD128(rWd, lane * 16, so, FF_AUX); rb[i] = LD32(rWd, 1024 + lane * 4, so, FF_AUX); }
    else { const unsigned so = (h * 128 + 16 * w + (i - 4)) * 1280; ra[i] = LD128(rWo, lane * 16, so, WO_AUX); rb[i] = LD32(rWo, 1024 + lane * 4, so, WO_AUX); }
    SB();
  };
  sfor<32>([&](auto T) __attribute__((always_inline)) {
    constexpr int t = T;
    constexpr int nx = icum(t) > icum(t - 1) ? icum(t - 1) : 32;
    if constexpr (t == 16) {
      STAMP(4);
      if constexpr (PTP == 1) untouch(IC<PT & 7>{});
      CB();
      const f32x4 s4 = *(const f32x4*)&sm.sc[w][lane * 4];
      mw = wave_max(fmaxf(fmaxf(s4[0], s4[1]), fmaxf(s4[2], s4[3])));
      const float p0 = exp_(s4[0] - mw), p1 = exp_(s4[1] - mw), p2 = exp_(s4[2] - mw), p3 = exp_(s4[3] - mw);
      const unsigned q01 = pk(p0, p1), q23 = pk(p2, p3);
      u32x4 pw = {q01 & 0xffffu, q01 >> 16, q23 & 0xffffu, q23 >> 16};
      *(u32x4*)&sm.sc[w][lane * 4] = pw;
      const float lw = wave_sum((p0 + p1) + (p2 + p3));
      u32x4 g = {f2u(mw), tagA, f2u(lw), tagB};
      ST128(g, rWs, lane == 0 ? 0u : 0x80000000u, WS_ML + (b * H + h) * 256 + (half * 8 + w) * 16);
      CB();
      SB();
    }
    if constexpr (t == 31) {
      if (__builtin_expect(!__all(ml[1] == tagA && ml[3] == tagB), 0)) {
        unsigned spins = 0;
        do {
          __builtin_amdgcn_s_sleep(2);
          CB();
          ml = LD128(rWs, c * 16, WS_ML + (b * H + h) * 256, SC1);
        } while (!__all(ml[1] == tagA && ml[3] == tagB) && ++spins < SPIN_CAP);
      }
      const float mi = u2f(ml[0]), M = row_max(mi);
      const float L = row_sum(u2f(ml[2]) * exp_(mi - M));
      fw = exp_(mw - M) / L;
      asm volatile("" : "+v"(fw));
      SB();
    }
    if constexpr (t < 16) {
      float a[4];
      sfor<4>([&](auto J) __attribute__((always_inline)) {
        constexpr int j = J;
        if constexpr (EARLY) { asm volatile("" :: "v"(kv[t][j])); SB(); ld_kv1(IC<nx>{}, J); }
        float u = dot2(kv[t][j][0], qp[0], 0.f); u = dot2(kv[t][j][1], qp[1], u); u = dot2(kv[t][j][2], qp[2], u); a[j] = dot2(kv[t][j][3], qp[3], u);
        SB();
        if constexpr (!EARLY) ld_kv1(IC<nx>{}, J);
      });
      const float u0 = (b3 ? a[1] : a[0]) + dpp<ROR8>(b3 ? a[0] : a[1]);
      const float u1 = (b3 ? a[3] : a[2]) + dpp<ROR8>(b3 ? a[2] : a[3]);
      float v = (b2 ? u1 : u0) + dpp<HMIR>(b2 ? u0 : u1);
      v += dpp<XOR1>(v); v += dpp<XOR2>(v);
      scw[t * 16] = rbf(rbf(v) * 0.08838834764831845f);
    } else {
      const u32x4 pv = *(const u32x4*)(pr + (t - 16) * 16);
      sfor<4>([&](auto J) __attribute__((always_inline)) {
        constexpr int j = J;
        if constexpr (EARLY) { asm volatile("" :: "v"(kv[t][j])); SB(); ld_kv1(IC<nx>{}, J); }
        const unsigned pl = pv[j], ph = pl << 16;
#pragma unroll
        for (int k = 0; k < 4; ++k) { acc[2 * k] = dot2(kv[t][j][k], pl, acc[2 * k]); acc[2 * k + 1] = dot2(kv[t][j][k], ph, acc[2 * k + 1]); }
        SB();
        if constexpr (!EARLY) ld_kv1(IC<nx>{}, J);
      });
#pragma unroll
      for (int k = 0; k < 8; ++k) asm volatile("" : "+v"(acc[k]));
    }
    SB();
    if constexpr (t == PK - 1) STAMP(3);
    if constexpr (icum(t) > icum(t - 1) + 1) sfor<icum(t) - icum(t - 1) - 1>([&](auto I) __attribute__((always_inline)) { ld_kv(IC<icum(t - 1) + 1 + I>{}); });
    sfor<xcum(t + 1) - xcum(t)>([&](auto I) __attribute__((always_inline)) {
      constexpr int i = xcum(t) + I;
      if constexpr (i >= 10) ld_row(IC<i - 10>{});
      else if constexpr (i & 1) { uv[i / 2] = LD128(rWu, gu_vo, gu_so + (i / 2) * 3456, FF_AUX); SB(); }
      else { gv[i / 2] = LD128(rWg, gu_vo, gu_so + (i / 2) * 3456, FF_AUX); SB(); }
    });
    if constexpr (t == T_ML) { ml = LD128(rWs, c * 16, WS_ML + (b * H + h) * 256, SC1); SB(); }
    if constexpr (t == T_FF) {
      const unsigned short* xs = (const unsigned short*)&sm.xn[w][0] + 80 * w + 5 * (lane >> 2);
      float a[16];
#pragma unroll
      for (int k = 0; k < 16; ++k) a[k] = 0.f;
#pragma unroll
      for (int i = 0; i < 5; ++i) {
        const unsigned xl = xs[i], xh = xl << 16;
#pragma unroll
        for (int k = 0; k < 4; ++k) {
          a[2 * k] = dot2(gv[i][k], xl, a[2 * k]); a[2 * k + 1] = dot2(gv[i][k], xh, a[2 * k + 1]);
          a[8 + 2 * k] = dot2(uv[i][k], xl, a[8 + 2 * k]); a[9 + 2 * k] = dot2(uv[i][k], xh, a[9 + 2 * k]);
        }
      }
      float e[8];
#pragma unroll
      for (int k = 0; k < 8; ++k) e[k] = swap32_add(a[k], a[k + 8]);
      f32x4 f;
#pragma unroll
      for (int k = 0; k < 4; ++k) { float u = swap16_add(e[k], e[k + 4]); u += dpp<ROR4>(u); u += dpp<ROR8>(u); f[k] = u; }
      *(f32x4*)&sm.fpart[w][(lane >> 5) * 32 + (lane & 3) * 8 + ((lane >> 4) & 1) * 4] = f;
      CB();
      if (lane == 0) __hip_atomic_fetch_add(&sm.cnt, 1u, __ATOMIC_RELAXED, __HIP_MEMORY_SCOPE_WORKGROUP);
      CB();
      SB();
    }
    if constexpr (t == T_H) {
      unsigned spins = 0;
      while (__builtin_amdgcn_readfirstlane(__hip_atomic_load(&sm.cnt, __ATOMIC_RELAXED, __HIP_MEMORY_SCOPE_WORKGROUP)) != 8u && ++spins < SPIN_CAP)
        __builtin_amdgcn_s_sleep(1);
      CB();
      const float fsum = sm.fpart[lane >> 3][((lane >> 2) & 1) * 32 + 4 * w + (lane & 3)];
      float v = fsum + dpp<ROR8>(fsum);
      v = rbf(xrow_sum(v));
      const float gs = rbf(v / (1.f + exp_(-v)));
      const float hh = gs * dpp<SHL4>(v);
      const unsigned hp = pk(hh, dpp<XOR1>(hh));
      const unsigned h01 = __builtin_amdgcn_readlane(hp, 0), h23 = __builtin_amdgcn_readlane(hp, 2);
#pragma unroll
      for (int k = 0; k < 4; ++k) {
        y[2 * k] = dot2(plo(ra[2][k], ra[3][k]), h23, dot2(plo(ra[0][k], ra[1][k]), h01, 0.f));
        y[2 * k + 1] = dot2(phi(ra[2][k], ra[3][k]), h23, dot2(phi(ra[0][k], ra[1][k]), h01, 0.f));
      }
      y[8] = dot2(plo(rb[2], rb[3]), h23, dot2(plo(rb[0], rb[1]), h01, 0.f));
      y[9] = dot2(phi(rb[2], rb[3]), h23, dot2(phi(rb[0], rb[1]), h01, 0.f));
#pragma unroll
      for (int k = 0; k < 10; ++k) asm volatile("" : "+v"(y[k]));
      SB();
    }
    sfor<pcum(t + 1) - pcum(t)>([&](auto I) __attribute__((always_inline)) {
      constexpr int i = pcum(t) + I;
#pragma unroll
      for (int k = 0; k < 4; ++k) { wp[i][2 * k] = plo(ra[4 + 2 * i][k], ra[5 + 2 * i][k]); wp[i][2 * k + 1] = phi(ra[4 + 2 * i][k], ra[5 + 2 * i][k]); }
      wp[i][8] = plo(rb[4 + 2 * i], rb[5 + 2 * i]); wp[i][9] = phi(rb[4 + 2 * i], rb[5 + 2 * i]);
#pragma unroll
      for (int k = 0; k < 10; ++k) asm volatile("" : "+v"(wp[i][k]));
      SB();
    });
  });
  STAMP(5);
  {
    float e[4];
#pragma unroll
    for (int k = 0; k < 4; ++k) e[k] = swap32_add(acc[k], acc[k + 4]);
    f32x2 g = {swap16_add(e[0], e[2]) * fw, swap16_add(e[1], e[3]) * fw};
    *(f32x2*)&sm.opart[w][8 * c + 2 * r] = g;
  }
  BARRIER();
  {
    float v = sm.opart[2 * r][16 * w + c] + sm.opart[2 * r + 1][16 * w + c];
    v = xrow_sum(v);
    const unsigned op = pk(v, dpp<XOR1>(v));
#pragma unroll
    for (int i = 0; i < 8; ++i) {
      const unsigned oi = __builtin_amdgcn_readlane(op, 2 * i);
#pragma unroll
      for (int k = 0; k < 10; ++k) y[k] = dot2(wp[i][k], oi, y[k]);
    }
    f32x4 y0 = {y[0], y[1], y[2], y[3]}, y1 = {y[4], y[5], y[6], y[7]};
    f32x2 y2 = {y[8], y[9]};
    *(f32x4*)&sm.ypart[w][lane * 4] = y0;
    *(f32x4*)&sm.ypart[w][256 + lane * 4] = y1;
    *(f32x2*)&sm.ypart[w][512 + lane * 2] = y2;
  }
  BARRIER();
  if (w < 5) {
    f32x2 s = *(const f32x2*)&sm.ypart[0][2 * tid];
#pragma unroll
    for (int i = 1; i < 8; ++i) s += *(const f32x2*)&sm.ypart[i][2 * tid];
    u32x4 g = {f2u(s[0]), tagA, f2u(s[1]), tagB};
    ST128(g, rWs, tid * 16, WS_Y + (b * 64 + h * 2 + half) * 5120);
  }
  STAMP(6);
  if (!is_red) return;
  const unsigned xo = LD32(rX, ocol * 2, b * 1280, 0);
  {
    u32x4 g[8];
    const unsigned so = WS_Y + (b * 64 + w * 8) * 5120 + rj * 1024;
#pragma unroll
    for (int i = 0; i < 8; ++i) g[i] = LD128(rWs, lane * 16, so + i * 5120, SC1);
    unsigned spins = 0;
    for (;;) {
      bool all = true;
      CB();
#pragma unroll
      for (int i = 0; i < 8; ++i) {
        if (!__all(g[i][1] == tagA && g[i][3] == tagB)) { all = false; g[i] = LD128(rWs, lane * 16, so + i * 5120, SC1); }
      }
      if (all || ++spins > SPIN_CAP) break;
      __builtin_amdgcn_s_sleep(1);
    }
    f32x2 s = {0.f, 0.f};
#pragma unroll
    for (int i = 0; i < 8; ++i) { s[0] += u2f(g[i][0]); s[1] += u2f(g[i][2]); }
    *(f32x2*)&sm.red[w][2 * lane] = s;
  }
  BARRIER();
  if (w == 0) {
    f32x2 s = *(const f32x2*)&sm.red[0][2 * lane];
#pragma unroll
    for (int i = 1; i < 8; ++i) s += *(const f32x2*)&sm.red[i][2 * lane];
    *(unsigned*)(out + b * D + ocol) = pk(blo(xo) + s[0], bhi(xo) + s[1]);
    if (rj == 0 && lane == 0) {
      u32x2 e = {tagA, tagB};
      __builtin_amdgcn_raw_buffer_store_b64(e, rWs, 0, WS_EPOCH + b * 256, SC1);
    }
  }
  STAMP(7);
  if constexpr (CPF) asm volatile(".Lpf_end:");
}

extern "C" void kernel_launch(void* const* d_in, const int* in_sizes, int n_in, void* d_out, int out_size, void* d_ws, size_t ws_size,
                              hipStream_t stream) {
  fused_decode<<<256, 512, 0, stream>>>((const unsigned short*)d_in[0], (const unsigned short*)d_in[1], (const unsigned short*)d_in[2],
                                        (const unsigned short*)d_in[3], (const unsigned short*)d_in[4], (const unsigned short*)d_in[6],
                                        (const unsigned short*)d_in[7], (const unsigned short*)d_in[8], (unsigned short*)d_out,
                                        (unsigned char*)d_ws);
}
```

```cpp
#include <hip/hip_runtime.h>
#include <hip/hip_bf16.h>
#include <stdint.h>
#include <utility>

typedef unsigned u32x4 __attribute__((ext_vector_type(4)));
typedef unsigned u32x2 __attribute__((ext_vector_type(2)));
typedef float f32x4 __attribute__((ext_vector_type(4)));
typedef float f32x2 __attribute__((ext_vector_type(2)));
typedef __bf16 bf16x2 __attribute__((ext_vector_type(2)));

#define W_AUX 0
#define WQ_AUX W_AUX
#define WO_AUX W_AUX
#define FF_AUX W_AUX
#define K_AUX 2
#define V_AUX 2
constexpr int SC1 = 16;
constexpr int D = 640, H = 32, F = 1728;
constexpr unsigned WS_EPOCH = 0, WS_ML = 4096, WS_Y = 65536, WS_BYTES = WS_Y + 4 * 64 * 5120;
constexpr unsigned SPIN_CAP = 1u << 18;

#define LD128(r, vo, so, aux) __builtin_amdgcn_raw_buffer_load_b128(r, vo, so, aux)
#define LD64(r, vo, so, aux) __builtin_amdgcn_raw_buffer_load_b64(r, vo, so, aux)
#define LD32(r, vo, so, aux) __builtin_amdgcn_raw_buffer_load_b32(r, vo, so, aux)
#define SB() __builtin_amdgcn_sched_barrier(0)
#define ST128(v, r, vo, so) do { __builtin_amdgcn_raw_buffer_store_b128(v, r, vo, so, SC1); SB(); asm volatile("s_nop 1"); SB(); } while (0)
#define RING 1
#define RSB() __builtin_amdgcn_sched_barrier(0)
#define CB() asm volatile("" ::: "memory")
#define BARRIER() asm volatile("s_waitcnt lgkmcnt(0)\n\ts_barrier" ::: "memory")

__device__ __forceinline__ float u2f(unsigned u) { return __builtin_bit_cast(float, u); }
__device__ __forceinline__ unsigned f2u(float f) { return __builtin_bit_cast(unsigned, f); }
__device__ __forceinline__ float blo(unsigned u) { return u2f(u << 16); }
__device__ __forceinline__ float bhi(unsigned u) { return u2f(u & 0xffff0000u); }
__device__ __forceinline__ unsigned pk(float lo, float hi) {
  f32x2 v = {lo, hi};
  return __builtin_bit_cast(unsigned, __builtin_convertvector(v, bf16x2));
}
__device__ __forceinline__ float rbf(float f) { return blo(pk(f, f)); }
__device__ __forceinline__ float dot2(unsigned a, unsigned b, float acc) {
  return __builtin_amdgcn_fdot2_f32_bf16(__builtin_bit_cast(bf16x2, a), __builtin_bit_cast(bf16x2, b), acc, false);
}
template <int CTRL> __device__ __forceinline__ float dpp(float x) {
  return u2f((unsigned)__builtin_amdgcn_mov_dpp((int)f2u(x), CTRL, 0xf, 0xf, true));
}
constexpr int XOR1 = 0xB1, XOR2 = 0x4E, HMIR = 0x141, ROR8 = 0x128, ROR4 = 0x124, SHL4 = 0x104;
__device__ __forceinline__ float swap32_add(float a, float b) {
  auto t = __builtin_amdgcn_permlane32_swap(f2u(a), f2u(b), false, false);
  return u2f(t[0]) + u2f(t[1]);
}
__device__ __forceinline__ float swap16_add(float a, float b) {
  auto t = __builtin_amdgcn_permlane16_swap(f2u(a), f2u(b), false, false);
  return u2f(t[0]) + u2f(t[1]);
}
__device__ __forceinline__ float xrow_sum(float x) { x = swap16_add(x, x); return swap32_add(x, x); }
__device__ __forceinline__ float xrow_max(float x) {
  auto s = __builtin_amdgcn_permlane16_swap(f2u(x), f2u(x), false, false);
  x = fmaxf(u2f(s[0]), u2f(s[1]));
  auto t = __builtin_amdgcn_permlane32_swap(f2u(x), f2u(x), false, false);
  return fmaxf(u2f(t[0]), u2f(t[1]));
}
__device__ __forceinline__ float row_sum(float v) { v += dpp<XOR1>(v); v += dpp<XOR2>(v); v += dpp<HMIR>(v); v += dpp<ROR8>(v); return v; }
__device__ __forceinline__ float row_max(float v) {
  v = fmaxf(v, dpp<XOR1>(v)); v = fmaxf(v, dpp<XOR2>(v)); v = fmaxf(v, dpp<HMIR>(v)); v = fmaxf(v, dpp<ROR8>(v)); return v;
}
__device__ __forceinline__ float wave_sum(float v) { return xrow_sum(row_sum(v)); }
__device__ __forceinline__ float wave_max(float v) { return xrow_max(row_max(v)); }
__device__ __forceinline__ unsigned plo(unsigned a, unsigned b) { return __builtin_amdgcn_perm(b, a, 0x05040100u); }
__device__ __forceinline__ unsigned phi(unsigned a, unsigned b) { return __builtin_amdgcn_perm(b, a, 0x07060302u); }
__device__ __forceinline__ float exp_(float x) { return __builtin_amdgcn_exp2f(x * 1.4426950408889634f); }

#define STAMP(i) do { } while (0)

template <int N> struct IC { static constexpr int value = N; constexpr operator int() const { return N; } };
template <typename Fn, int... I> __device__ __forceinline__ void sfor_impl(Fn&& f, std::integer_sequence<int, I...>) { (f(IC<I>{}), ...); }
template <int N, typename Fn> __device__ __forceinline__ void sfor(Fn&& f) { sfor_impl(f, std::make_integer_sequence<int, N>{}); }
#define ROT 1
#define EARLY 1
#define EPS 1
#define XOL 1
#define PT 0
#define PTP 0
#define VT 0
#define CPF 1
#define HMAP 1
#define PSEQ "wwwKwwwKwwwKwwwKwwwKwwwKwwK"
#define WN 8
#define XB 2
#define T_D2 32
#define T_D3 32
constexpr char pseq[] = PSEQ;
constexpr int PLEN = sizeof(pseq) - 1;
constexpr int pcount(int n, char ch) { int a = 0; for (int i = 0; i < n; ++i) a += pseq[i] == ch; return a; }
constexpr int PK = pcount(PLEN, 'K');
constexpr int PNL = 20 + 4 * PK;
static_assert(pcount(PLEN, 'w') == 20, "20 wq loads");
constexpr int pload(int n) { int k = 0; for (int i = 0; i < PLEN; ++i) { const int len = pseq[i] == 'w' ? 1 : 4; if (n < k + len) return pseq[i] == 'w' ? pcount(i, 'w') : -(1 + 4 * pcount(i, 'K') + (n - k)); k += len; } return 0; }
constexpr int wq_done(int n) { int a = 0; for (int i = 0; i < n && i < PNL; ++i) a += pload(i) >= 0; return a / 2; }
constexpr int icum(int t) {
  int v = PK + (XB >= 1 ? 1 : 0);
  if (XB >= 2 && t >= PK / 2) v = PK + 2;
  if (XB >= 3 && t >= PK - 1) v = PK + 3;
  const int d = t >= T_D3 ? 3 : (t >= T_D2 ? 2 : 1);
  if (t + 1 + d > v) v = t + 1 + d;
  return v > 32 ? 32 : v;
}
#define T_X0 (PK + XB - 1)
#define T_X1 29
#define GUW 8
constexpr int xstep(int i) { const int cw = i < 10 ? GUW * i : GUW * 10 + 5 * (i - 10); return T_X0 + cw * (T_X1 - T_X0) / (GUW * 10 + 100); }
constexpr int xcum(int t) { int a = 0; for (int i = 0; i < 30; ++i) a += xstep(i) < t; return a; }
constexpr int pcum(int t) { const int n = xcum(t - 2) - 14; return n < 0 ? 0 : n / 2; }
constexpr int T_FF = xstep(9) + 2;
constexpr int T_H = xstep(13) + 2 > T_FF + 3 ? xstep(13) + 2 : T_FF + 3;
constexpr int T_ML = 28;
static_assert(pcum(32) == 8 && T_H < 31, "schedule must finish inside the stream");
constexpr int vmcnt_imm(int n) { return (n & 15) | ((n >> 4) << 14) | 0x0F70; }

struct Smem {
  unsigned xn[8][320];
  float qpart[8][128];
  float fpart[8][64];
  unsigned qv[8][64];
  float sc[8][256];
  float opart[8][128];
  float ypart[8][640];
  float red[8][128];
  unsigned cnt;
};

__global__ void __launch_bounds__(512, 2)
fused_decode(const unsigned short* __restrict__ x, const unsigned short* __restrict__ kc, const unsigned short* __restrict__ vc,
             const unsigned short* __restrict__ wq, const unsigned short* __restrict__ wo, const unsigned short* __restrict__ wg,
             const unsigned short* __restrict__ wu, const unsigned short* __restrict__ wd, unsigned short* __restrict__ out,
             unsigned char* ws) {
  __shared__ __attribute__((aligned(16))) Smem sm;
  const int tid = threadIdx.x, lane = tid & 63;
  const int w = __builtin_amdgcn_readfirstlane(tid >> 6);
  const int bid = blockIdx.x, x8 = bid & 7, rr_ = bid >> 3, half = rr_ & 1, b = (rr_ >> 1) & 3, hhi = rr_ >> 3, h = HMAP ? x8 * 4 + hhi : hhi * 8 + x8;
  const int r = lane >> 4, c = lane & 15;
  STAMP(0);
  asm volatile("" :: "s"(x), "s"(kc), "s"(vc), "s"(wq), "s"(wo), "s"(wg), "s"(wu), "s"(wd), "s"(out), "s"(ws));

  const unsigned region = (unsigned)((b * H + h) * 2 + half);
  auto rX = __builtin_amdgcn_make_buffer_rsrc((void*)x, 0, 4 * D * 2, 0x00020000);
  auto rK = __builtin_amdgcn_make_buffer_rsrc((void*)(kc + (size_t)region * (2048 * 128)), 0, 512 * 1024, 0x00020000);
  auto rV = __builtin_amdgcn_make_buffer_rsrc((void*)(vc + (size_t)region * (2048 * 128)), 0, 512 * 1024, 0x00020000);
  auto rWq = __builtin_amdgcn_make_buffer_rsrc((void*)wq, 0, D * 4096 * 2, 0x00020000);
  auto rWo = __builtin_amdgcn_make_buffer_rsrc((void*)wo, 0, D * 4096 * 2, 0x00020000);
  auto rWs = __builtin_amdgcn_make_buffer_rsrc((void*)ws, 0, WS_BYTES, 0x00020000);
  const int slot = hhi * 2 + half, fcol = (x8 < 3 ? 4 * x8 : 3 * x8 + 3) * 64 + slot * 32;
  const bool has_ffn = slot < (x8 < 3 ? 8 : 6);
  auto rWg = __builtin_amdgcn_make_buffer_rsrc((void*)wg, 0, has_ffn ? D * F * 2 : 0, 0x00020000);
  auto rWu = __builtin_amdgcn_make_buffer_rsrc((void*)wu, 0, has_ffn ? D * F * 2 : 0, 0x00020000);
  auto rWd = __builtin_amdgcn_make_buffer_rsrc((void*)wd, 0, has_ffn ? F * D * 2 : 0, 0x00020000);
  const int rj = (h - b * 5) & 31;
  const bool is_red = (half == 0) && (rj < 5);
  const int gp = rj * 64 + lane;
  const int ocol = gp < 128 ? 8 * (gp >> 1) + 2 * (gp & 1) : (gp < 256 ? 8 * ((gp - 128) >> 1) + 4 + 2 * (gp & 1) : 512 + 2 * (gp - 256));

  u32x4 cpf;
  if constexpr (CPF) {
    unsigned long long pc; unsigned len;
    asm volatile("s_getpc_b64 %0\n.Lpf_begin:\n\ts_mov_b32 %1, .Lpf_end-.Lpf_begin" : "=s"(pc), "=s"(len));
    auto rC = __builtin_amdgcn_make_buffer_rsrc((void*)(pc & ~15ull), 0, w == 0 ? len : 0, 0x00020000);
    cpf = LD128(rC, lane * 16 + rr_ * 1024, 0, 0);
    SB();
  }
  const u32x4 xa = LD128(rX, lane * 16, b * 1280, 0);
  const unsigned xb = LD32(rX, 1024 + lane * 4, b * 1280, 0);
  const u32x2 ep = *(const __attribute__((address_space(4))) u32x2*)((unsigned long long)ws + WS_EPOCH + b * 256);
  if (tid == 0) sm.cnt = 0;
  SB();
  unsigned pt[5];
  auto touch = [&](auto Q) __attribute__((always_inline)) {
    constexpr int q = Q;
    const unsigned wi = rr_ * 8 + w, nl = x8 < 3 ? 4 : 3, f0 = (x8 < 3 ? 4 * x8 : 3 * x8 + 3) * 64, off = 0x80000000u;
    if constexpr (q & 1) { pt[0] = LD32(rWo, lane < 40 ? lane * 64 : off, (x8 * 256 + wi) * 2560, WO_AUX); SB(); }
    if constexpr (q & 2) {
      auto rA = __builtin_amdgcn_make_buffer_rsrc((void*)wd, 0, F * D * 2, 0x00020000);
      pt[1] = LD32(rA, lane < 5 * nl ? lane * 64 : off, f0 * 1280 + wi * nl * 320, FF_AUX); SB();
    }
    if constexpr (q & 4) {
      auto rA = __builtin_amdgcn_make_buffer_rsrc((void*)wg, 0, F * D * 2, 0x00020000);
      auto rB = __builtin_amdgcn_make_buffer_rsrc((void*)wu, 0, F * D * 2, 0x00020000);
      const unsigned ch = wi * 20 + lane, vo = lane < 20 && (ch & 7) < 2 * nl ? (ch >> 3) * 3456 + (ch & 7) * 64 : off;
      pt[2] = LD32(rA, vo, f0 * 2, FF_AUX); SB();
      pt[3] = LD32(rB, vo, f0 * 2, FF_AUX); SB();
    }
    if constexpr (q & 8) {
      const unsigned ch = wi * 40 + lane;
      pt[4] = LD32(rWq, lane < 40 ? (ch >> 4) * 8192 + (ch & 15) * 64 : off, x8 * 1024, WQ_AUX); SB();
    }
  };
  auto untouch = [&](auto Q) __attribute__((always_inline)) {
    constexpr int q = Q;
    if constexpr (q & 1) asm volatile("" :: "v"(pt[0]));
    if constexpr (q & 2) asm volatile("" :: "v"(pt[1]));
    if constexpr (q & 4) asm volatile("" :: "v"(pt[2]), "v"(pt[3]));
    if constexpr (q & 8) asm volatile("" :: "v"(pt[4]));
  };
  static_assert(HMAP == 1 || PT == 0, "prefetch map");
  unsigned vt0, vt1;
  if constexpr (VT > 0) {
    vt0 = LD32(rK, lane < VT ? lane * (65536 / VT) : 0x80000000u, w * 65536, K_AUX); SB();
    vt1 = LD32(rV, lane < VT ? lane * (65536 / VT) : 0x80000000u, w * 65536, V_AUX); SB();
  }
  touch(IC<PT & 8>{});
  if constexpr (PTP == 0) touch(IC<PT & 7>{});
  u32x4 wqv[20];
  u32x4 kv[33][4];
  const unsigned kvo = lane * 16;
  const int rot = ROT == 0 ? 0 : (ROT == 1 ? rr_ : (ROT == 2 ? 2 * x8 : rr_ + 2 * x8 + w));
  const unsigned wq_vo = r * (20 * 8192) + c * 16, wq_so = w * (80 * 8192) + h * 256;
  auto ld_kv1 = [&](auto T, auto J) __attribute__((always_inline)) {
    constexpr int t = T, j = J;
    if constexpr (t < 16) kv[t][j] = LD128(rK, kvo + j * 1024, (((t + rot) & 15) * 8 + w) * 4096, K_AUX);
    else if constexpr (t < 32) kv[t][j] = LD128(rV, kvo + j * 1024, (((t + rot) & 15) * 8 + w) * 4096, V_AUX);
    SB();
  };
  auto ld_kv = [&](auto T) __attribute__((always_inline)) { sfor<4>([&](auto J) __attribute__((always_inline)) { ld_kv1(T, J); }); };
  auto ld_pro = [&](auto N) __attribute__((always_inline)) {
    constexpr int p = pload(N);
    if constexpr (p >= 0) { wqv[p] = LD128(rWq, wq_vo, wq_so + p * 8192, WQ_AUX); SB(); }
    else ld_kv1(IC<(-p - 1) / 4>{}, IC<(-p - 1) % 4>{});
  };
  sfor<WN>([&](auto N) __attribute__((always_inline)) { ld_pro(N); });
  {
    float v[10] = {blo(xa[0]), bhi(xa[0]), blo(xa[1]), bhi(xa[1]), blo(xa[2]), bhi(xa[2]), blo(xa[3]), bhi(xa[3]), blo(xb), bhi(xb)};
    float s = 0.f;
#pragma unroll
    for (int i = 0; i < 10; ++i) s += v[i];
    const float mean = wave_sum(s) * (1.f / D);
    float s2 = 0.f;
#pragma unroll
    for (int i = 0; i < 10; ++i) { const float d = v[i] - mean; s2 += d * d; }
    const float var = wave_sum(s2) * (1.f / D);
    const float mb = rbf(mean), den = rbf(__builtin_sqrtf(rbf(rbf(var) + 1e-5f))), rd = 1.f / den;
#pragma unroll
    for (int i = 0; i < 10; ++i) v[i] = rbf(v[i] - mb) * rd;
    u32x4 o4 = {pk(v[0], v[1]), pk(v[2], v[3]), pk(v[4], v[5]), pk(v[6], v[7])};
    *(u32x4*)&sm.xn[w][lane * 4] = o4;
    sm.xn[w][256 + lane] = pk(v[8], v[9]);
  }
  CB();
  {
    unsigned xp[10];
    const unsigned* xs = &sm.xn[w][40 * w + 10 * r];
#pragma unroll
    for (int i = 0; i < 5; ++i) { const u32x2 t = *(const u32x2*)(xs + 2 * i); xp[2 * i] = t[0]; xp[2 * i + 1] = t[1]; }
    float a[8] = {0.f, 0.f, 0.f, 0.f, 0.f, 0.f, 0.f, 0.f};
    SB();
    auto qpair = [&](auto I) __attribute__((always_inline)) {
      constexpr int i = I;
#pragma unroll
      for (int k = 0; k < 4; ++k) {
        a[2 * k] = dot2(plo(wqv[2 * i][k], wqv[2 * i + 1][k]), xp[i], a[2 * k]);
        a[2 * k + 1] = dot2(phi(wqv[2 * i][k], wqv[2 * i + 1][k]), xp[i], a[2 * k + 1]);
      }
      SB();
    };
    sfor<PNL - WN>([&](auto I) __attribute__((always_inline)) {
      constexpr int n = I + WN;
      __builtin_amdgcn_s_waitcnt(vmcnt_imm(WN - 1));
      SB();
      ld_pro(IC<n>{});
      sfor<wq_done(n - WN + 1) - wq_done(n - WN)>([&](auto J) __attribute__((always_inline)) { qpair(IC<wq_done(n - WN) + J>{}); });
    });
    sfor<10 - wq_done(PNL - WN)>([&](auto J) __attribute__((always_inline)) { qpair(IC<wq_done(PNL - WN) + J>{}); });
    float e[4];
#pragma unroll
    for (int k = 0; k < 4; ++k) e[k] = swap32_add(a[k], a[k + 4]);
    f32x2 g = {swap16_add(e[0], e[2]), swap16_add(e[1], e[3])};
    *(f32x2*)&sm.qpart[w][8 * c + 2 * r] = g;
  }
  if constexpr (CPF) asm volatile("" :: "v"(cpf));
  if constexpr (VT > 0) asm volatile("" :: "v"(vt0), "v"(vt1));
  untouch(IC<PTP == 0 ? PT : (PT & 8)>{});
  STAMP(1);
  BARRIER();
  if constexpr (PTP == 1) touch(IC<PT & 7>{});
  sfor<icum(-1) - PK>([&](auto I) __attribute__((always_inline)) { ld_kv(IC<PK + I>{}); });
  u32x4 qp;
  {
    f32x2 s = *(const f32x2*)&sm.qpart[0][2 * lane];
#pragma unroll
    for (int i = 1; i < 8; ++i) s += *(const f32x2*)&sm.qpart[i][2 * lane];
    sm.qv[w][lane] = pk(s[0], s[1]);
    CB();
    qp = *(const u32x4*)&sm.qv[w][4 * c];
  }
  STAMP(2);

  const bool b3 = lane & 8, b2 = lane & 4;
  float* scw = &sm.sc[w][r * 4 + ((lane >> 3) & 1) + ((lane >> 1) & 2)];
  const unsigned* pr = (const unsigned*)&sm.sc[w][r * 4];
  const unsigned gu_vo = (lane >> 2) * (5 * 3456) + (lane & 3) * 16, gu_so = w * (80 * 3456) + fcol * 2;
  u32x4 gv[5], uv[5];
  u32x4 ra[20]; unsigned rb[20];
  unsigned wp[8][10];
  float y[10];
  float acc[8] = {0.f, 0.f, 0.f, 0.f, 0.f, 0.f, 0.f, 0.f};
  float mw = 0.f, fw = 0.f;
  u32x4 ml;
  unsigned tagA = ep[0] + 1u, tagB = ep[1] + 1u;
  asm volatile("" : "+s"(tagA), "+s"(tagB));
  auto ld_row = [&](auto R) __attribute__((always_inline)) {
    constexpr int i = R;
    if constexpr (i < 4) { const unsigned so = (fcol + 4 * w + i) * 1280; ra[i] = LD128(rWd, lane * 16, so, FF_AUX); rb[i] = LD32(rWd, 1024 + lane * 4, so, FF_AUX); }
    else { const unsigned so = (h * 128 + 16 * w + (i - 4)) * 1280; ra[i] = LD128(rWo, lane * 16, so, WO_AUX); rb[i] = LD32(rWo, 1024 + lane * 4, so, WO_AUX); }
    SB();
  };
  sfor<32>([&](auto T) __attribute__((always_inline)) {
    constexpr int t = T;
    constexpr int nx = icum(t) > icum(t - 1) ? icum(t - 1) : 32;
    if constexpr (t == 16) {
      STAMP(4);
      if constexpr (PTP == 1) untouch(IC<PT & 7>{});
      CB();
      const f32x4 s4 = *(const f32x4*)&sm.sc[w][lane * 4];
      mw = wave_max(fmaxf(fmaxf(s4[0], s4[1]), fmaxf(s4[2], s4[3])));
      const float p0 = exp_(s4[0] - mw), p1 = exp_(s4[1] - mw), p2 = exp_(s4[2] - mw), p3 = exp_(s4[3] - mw);
      const unsigned q01 = pk(p0, p1), q23 = pk(p2, p3);
      u32x4 pw = {q01 & 0xffffu, q01 >> 16, q23 & 0xffffu, q23 >> 16};
      *(u32x4*)&sm.sc[w][lane * 4] = pw;
      const float lw = wave_sum((p0 + p1) + (p2 + p3));
      u32x4 g = {f2u(mw), tagA, f2u(lw), tagB};
      ST128(g, rWs, lane == 0 ? 0u : 0x80000000u, WS_ML + (b * H + h) * 256 + (half * 8 + w) * 16);
      CB();
      SB();
    }
    if constexpr (t == 31) {
      if (__builtin_expect(!__all(ml[1] == tagA && ml[3] == tagB), 0)) {
        unsigned spins = 0;
        do {
          __builtin_amdgcn_s_sleep(2);
          CB();
          ml = LD128(rWs, c * 16, WS_ML + (b * H + h) * 256, SC1);
        } while (!__all(ml[1] == tagA && ml[3] == tagB) && ++spins < SPIN_CAP);
      }
      const float mi = u2f(ml[0]), M = row_max(mi);
      const float L = row_sum(u2f(ml[2]) * exp_(mi - M));
      fw = exp_(mw - M) / L;
      asm volatile("" : "+v"(fw));
      SB();
    }
    if constexpr (t < 16) {
      float a[4];
      sfor<4>([&](auto J) __attribute__((always_inline)) {
        constexpr int j = J;
        if constexpr (EARLY) { asm volatile("" :: "v"(kv[t][j])); SB(); ld_kv1(IC<nx>{}, J); }
        float u = dot2(kv[t][j][0], qp[0], 0.f); u = dot2(kv[t][j][1], qp[1], u); u = dot2(kv[t][j][2], qp[2], u); a[j] = dot2(kv[t][j][3], qp[3], u);
        SB();
        if constexpr (!EARLY) ld_kv1(IC<nx>{}, J);
      });
      const float u0 = (b3 ? a[1] : a[0]) + dpp<ROR8>(b3 ? a[0] : a[1]);
      const float u1 = (b3 ? a[3] : a[2]) + dpp<ROR8>(b3 ? a[2] : a[3]);
      float v = (b2 ? u1 : u0) + dpp<HMIR>(b2 ? u0 : u1);
      v += dpp<XOR1>(v); v += dpp<XOR2>(v);
      scw[t * 16] = rbf(rbf(v) * 0.08838834764831845f);
    } else {
      const u32x4 pv = *(const u32x4*)(pr + (t - 16) * 16);
      sfor<4>([&](auto J) __attribute__((always_inline)) {
        constexpr int j = J;
        if constexpr (EARLY) { asm volatile("" :: "v"(kv[t][j])); SB(); ld_kv1(IC<nx>{}, J); }
        const unsigned pl = pv[j], ph = pl << 16;
#pragma unroll
        for (int k = 0; k < 4; ++k) { acc[2 * k] = dot2(kv[t][j][k], pl, acc[2 * k]); acc[2 * k + 1] = dot2(kv[t][j][k], ph, acc[2 * k + 1]); }
        SB();
        if constexpr (!EARLY) ld_kv1(IC<nx>{}, J);
      });
#pragma unroll
      for (int k = 0; k < 8; ++k) asm volatile("" : "+v"(acc[k]));
    }
    SB();
    if constexpr (t == PK - 1) STAMP(3);
    if constexpr (icum(t) > icum(t - 1) + 1) sfor<icum(t) - icum(t - 1) - 1>([&](auto I) __attribute__((always_inline)) { ld_kv(IC<icum(t - 1) + 1 + I>{}); });
    sfor<xcum(t + 1) - xcum(t)>([&](auto I) __attribute__((always_inline)) {
      constexpr int i = xcum(t) + I;
      if constexpr (i >= 10) ld_row(IC<i - 10>{});
      else if constexpr (i & 1) { uv[i / 2] = LD128(rWu, gu_vo, gu_so + (i / 2) * 3456, FF_AUX); SB(); }
      else { gv[i / 2] = LD128(rWg, gu_vo, gu_so + (i / 2) * 3456, FF_AUX); SB(); }
    });
    if constexpr (t == T_ML) { ml = LD128(rWs, c * 16, WS_ML + (b * H + h) * 256, SC1); SB(); }
    if constexpr (t == T_FF) {
      const unsigned short* xs = (const unsigned short*)&sm.xn[w][0] + 80 * w + 5 * (lane >> 2);
      float a[16];
#pragma unroll
      for (int k = 0; k < 16; ++k) a[k] = 0.f;
#pragma unroll
      for (int i = 0; i < 5; ++i) {
        const unsigned xl = xs[i], xh = xl << 16;
#pragma unroll
        for (int k = 0; k < 4; ++k) {
          a[2 * k] = dot2(gv[i][k], xl, a[2 * k]); a[2 * k + 1] = dot2(gv[i][k], xh, a[2 * k + 1]);
          a[8 + 2 * k] = dot2(uv[i][k], xl, a[8 + 2 * k]); a[9 + 2 * k] = dot2(uv[i][k], xh, a[9 + 2 * k]);
        }
      }
      float e[8];
#pragma unroll
      for (int k = 0; k < 8; ++k) e[k] = swap32_add(a[k], a[k + 8]);
      f32x4 f;
#pragma unroll
      for (int k = 0; k < 4; ++k) { float u = swap16_add(e[k], e[k + 4]); u += dpp<ROR4>(u); u += dpp<ROR8>(u); f[k] = u; }
      *(f32x4*)&sm.fpart[w][(lane >> 5) * 32 + (lane & 3) * 8 + ((lane >> 4) & 1) * 4] = f;
      CB();
      if (lane == 0) __hip_atomic_fetch_add(&sm.cnt, 1u, __ATOMIC_RELAXED, __HIP_MEMORY_SCOPE_WORKGROUP);
      CB();
      SB();
    }
    if constexpr (t == T_H) {
      unsigned spins = 0;
      while (__builtin_amdgcn_readfirstlane(__hip_atomic_load(&sm.cnt, __ATOMIC_RELAXED, __HIP_MEMORY_SCOPE_WORKGROUP)) != 8u && ++spins < SPIN_CAP)
        __builtin_amdgcn_s_sleep(1);
      CB();
      const float fsum = sm.fpart[lane >> 3][((lane >> 2) & 1) * 32 + 4 * w + (lane & 3)];
      float v = fsum + dpp<ROR8>(fsum);
      v = rbf(xrow_sum(v));
      const float gs = rbf(v / (1.f + exp_(-v)));
      const float hh = gs * dpp<SHL4>(v);
      const unsigned hp = pk(hh, dpp<XOR1>(hh));
      const unsigned h01 = __builtin_amdgcn_readlane(hp, 0), h23 = __builtin_amdgcn_readlane(hp, 2);
#pragma unroll
      for (int k = 0; k < 4; ++k) {
        y[2 * k] = dot2(plo(ra[2][k], ra[3][k]), h23, dot2(plo(ra[0][k], ra[1][k]), h01, 0.f));
        y[2 * k + 1] = dot2(phi(ra[2][k], ra[3][k]), h23, dot2(phi(ra[0][k], ra[1][k]), h01, 0.f));
      }
      y[8] = dot2(plo(rb[2], rb[3]), h23, dot2(plo(rb[0], rb[1]), h01, 0.f));
      y[9] = dot2(phi(rb[2], rb[3]), h23, dot2(phi(rb[0], rb[1]), h01, 0.f));
#pragma unroll
      for (int k = 0; k < 10; ++k) asm volatile("" : "+v"(y[k]));
      SB();
    }
    sfor<pcum(t + 1) - pcum(t)>([&](auto I) __attribute__((always_inline)) {
      constexpr int i = pcum(t) + I;
#pragma unroll
      for (int k = 0; k < 4; ++k) { wp[i][2 * k] = plo(ra[4 + 2 * i][k], ra[5 + 2 * i][k]); wp[i][2 * k + 1] = phi(ra[4 + 2 * i][k], ra[5 + 2 * i][k]); }
      wp[i][8] = plo(rb[4 + 2 * i], rb[5 + 2 * i]); wp[i][9] = phi(rb[4 + 2 * i], rb[5 + 2 * i]);
#pragma unroll
      for (int k = 0; k < 10; ++k) asm volatile("" : "+v"(wp[i][k]));
      SB();
    });
  });
  STAMP(5);
  {
    float e[4];
#pragma unroll
    for (int k = 0; k < 4; ++k) e[k] = swap32_add(acc[k], acc[k + 4]);
    f32x2 g = {swap16_add(e[0], e[2]) * fw, swap16_add(e[1], e[3]) * fw};
    *(f32x2*)&sm.opart[w][8 * c + 2 * r] = g;
  }
  BARRIER();
  {
    float v = sm.opart[2 * r][16 * w + c] + sm.opart[2 * r + 1][16 * w + c];
    v = xrow_sum(v);
    const unsigned op = pk(v, dpp<XOR1>(v));
#pragma unroll
    for (int i = 0; i < 8; ++i) {
      const unsigned oi = __builtin_amdgcn_readlane(op, 2 * i);
#pragma unroll
      for (int k = 0; k < 10; ++k) y[k] = dot2(wp[i][k], oi, y[k]);
    }
    f32x4 y0 = {y[0], y[1], y[2], y[3]}, y1 = {y[4], y[5], y[6], y[7]};
    f32x2 y2 = {y[8], y[9]};
    *(f32x4*)&sm.ypart[w][lane * 4] = y0;
    *(f32x4*)&sm.ypart[w][256 + lane * 4] = y1;
    *(f32x2*)&sm.ypart[w][512 + lane * 2] = y2;
  }
  BARRIER();
  if (w < 5) {
    f32x2 s = *(const f32x2*)&sm.ypart[0][2 * tid];
#pragma unroll
    for (int i = 1; i < 8; ++i) s += *(const f32x2*)&sm.ypart[i][2 * tid];
    u32x4 g = {f2u(s[0]), tagA, f2u(s[1]), tagB};
    ST128(g, rWs, tid * 16, WS_Y + (b * 64 + h * 2 + half) * 5120);
  }
  STAMP(6);
  if (!is_red) return;
  const unsigned xo = LD32(rX, ocol * 2, b * 1280, 0);
  {
    u32x4 g[8];
    const unsigned so = WS_Y + (b * 64 + w * 8) * 5120 + rj * 1024;
#pragma unroll
    for (int i = 0; i < 8; ++i) g[i] = LD128(rWs, lane * 16, so + i * 5120, SC1);
    unsigned spins = 0;
    for (;;) {
      bool all = true;
      CB();
#pragma unroll
      for (int i = 0; i < 8; ++i) {
        if (!__all(g[i][1] == tagA && g[i][3] == tagB)) { all = false; g[i] = LD128(rWs, lane * 16, so + i * 5120, SC1); }
      }
      if (all || ++spins > SPIN_CAP) break;
      __builtin_amdgcn_s_sleep(1);
    }
    f32x2 s = {0.f, 0.f};
#pragma unroll
    for (int i = 0; i < 8; ++i) { s[0] += u2f(g[i][0]); s[1] += u2f(g[i][2]); }
    *(f32x2*)&sm.red[w][2 * lane] = s;
  }
  BARRIER();
  if (w == 0) {
    f32x2 s = *(const f32x2*)&sm.red[0][2 * lane];
#pragma unroll
    for (int i = 1; i < 8; ++i) s += *(const f32x2*)&sm.red[i][2 * lane];
    *(unsigned*)(out + b * D + ocol) = pk(blo(xo) + s[0], bhi(xo) + s[1]);
    if (rj == 0 && lane == 0) {
      u32x2 e = {tagA, tagB};
      __builtin_amdgcn_raw_buffer_store_b64(e, rWs, 0, WS_EPOCH + b * 256, SC1);
    }
  }
  STAMP(7);
  if constexpr (CPF) asm volatile(".Lpf_end:");
}

extern "C" void kernel_launch(void* const* d_in, const int* in_sizes, int n_in, void* d_out, int out_size, void* d_ws, size_t ws_size,
                              hipStream_t stream) {
  fused_decode<<<256, 512, 0, stream>>>((const unsigned short*)d_in[0], (const unsigned short*)d_in[1], (const unsigned short*)d_in[2],
                                        (const unsigned short*)d_in[3], (const unsigned short*)d_in[4], (const unsigned short*)d_in[6],
                                        (const unsigned short*)d_in[7], (const unsigned short*)d_in[8], (unsigned short*)d_out,
                                        (unsigned char*)d_ws);
}
```

```cpp
#include <hip/hip_runtime.h>
#include <hip/hip_bf16.h>
#include <stdint.h>
#include <utility>

typedef unsigned u32x4 __attribute__((ext_vector_type(4)));
typedef unsigned u32x2 __attribute__((ext_vector_type(2)));
typedef float f32x4 __attribute__((ext_vector_type(4)));
typedef float f32x2 __attribute__((ext_vector_type(2)));
typedef __bf16 bf16x2 __attribute__((ext_vector_type(2)));

#define W_AUX 0
#define WQ_AUX W_AUX
#define WO_AUX W_AUX
#define FF_AUX W_AUX
#define K_AUX 2
#define V_AUX 2
constexpr int SC1 = 16;
constexpr int D = 640, H = 32, F = 1728;
constexpr unsigned WS_EPOCH = 0, WS_ML = 4096, WS_Y = 65536, WS_BYTES = WS_Y + 4 * 64 * 5120;
constexpr unsigned SPIN_CAP = 1u << 18;

#define LD128(r, vo, so, aux) __builtin_amdgcn_raw_buffer_load_b128(r, vo, so, aux)
#define LD64(r, vo, so, aux) __builtin_amdgcn_raw_buffer_load_b64(r, vo, so, aux)
#define LD32(r, vo, so, aux) __builtin_amdgcn_raw_buffer_load_b32(r, vo, so, aux)
#define SB() __builtin_amdgcn_sched_barrier(0)
#define ST128(v, r, vo, so) do { __builtin_amdgcn_raw_buffer_store_b128(v, r, vo, so, SC1); SB(); asm volatile("s_nop 1"); SB(); } while (0)
#define RING 1
#define RSB() __builtin_amdgcn_sched_barrier(0)
#define CB() asm volatile("" ::: "memory")
#define BARRIER() asm volatile("s_waitcnt lgkmcnt(0)\n\ts_barrier" ::: "memory")

__device__ __forceinline__ float u2f(unsigned u) { return __builtin_bit_cast(float, u); }
__device__ __forceinline__ unsigned f2u(float f) { return __builtin_bit_cast(unsigned, f); }
__device__ __forceinline__ float blo(unsigned u) { return u2f(u << 16); }
__device__ __forceinline__ float bhi(unsigned u) { return u2f(u & 0xffff0000u); }
__device__ __forceinline__ unsigned pk(float lo, float hi) {
  f32x2 v = {lo, hi};
  return __builtin_bit_cast(unsigned, __builtin_convertvector(v, bf16x2));
}
__device__ __forceinline__ float rbf(float f) { return blo(pk(f, f)); }
__device__ __forceinline__ float dot2(unsigned a, unsigned b, float acc) {
  return __builtin_amdgcn_fdot2_f32_bf16(__builtin_bit_cast(bf16x2, a), __builtin_bit_cast(bf16x2, b), acc, false);
}
template <int CTRL> __device__ __forceinline__ float dpp(float x) {
  return u2f((unsigned)__builtin_amdgcn_mov_dpp((int)f2u(x), CTRL, 0xf, 0xf, true));
}
constexpr int XOR1 = 0xB1, XOR2 = 0x4E, HMIR = 0x141, ROR8 = 0x128, ROR4 = 0x124, SHL4 = 0x104;
__device__ __forceinline__ float swap32_add(float a, float b) {
  auto t = __builtin_amdgcn_permlane32_swap(f2u(a), f2u(b), false, false);
  return u2f(t[0]) + u2f(t[1]);
}
__device__ __forceinline__ float swap16_add(float a, float b) {
  auto t = __builtin_amdgcn_permlane16_swap(f2u(a), f2u(b), false, false);
  return u2f(t[0]) + u2f(t[1]);
}
__device__ __forceinline__ float xrow_sum(float x) { x = swap16_add(x, x); return swap32_add(x, x); }
__device__ __forceinline__ float xrow_max(float x) {
  auto s = __builtin_amdgcn_permlane16_swap(f2u(x), f2u(x), false, false);
  x = fmaxf(u2f(s[0]), u2f(s[1]));
  auto t = __builtin_amdgcn_permlane32_swap(f2u(x), f2u(x), false, false);
  return fmaxf(u2f(t[0]), u2f(t[1]));
}
__device__ __forceinline__ float row_sum(float v) { v += dpp<XOR1>(v); v += dpp<XOR2>(v); v += dpp<HMIR>(v); v += dpp<ROR8>(v); return v; }
__device__ __forceinline__ float row_max(float v) {
  v = fmaxf(v, dpp<XOR1>(v)); v = fmaxf(v, dpp<XOR2>(v)); v = fmaxf(v, dpp<HMIR>(v)); v = fmaxf(v, dpp<ROR8>(v)); return v;
}
__device__ __forceinline__ float wave_sum(float v) { return xrow_sum(row_sum(v)); }
__device__ __forceinline__ float wave_max(float v) { return xrow_max(row_max(v)); }
__device__ __forceinline__ unsigned plo(unsigned a, unsigned b) { return __builtin_amdgcn_perm(b, a, 0x05040100u); }
__device__ __forceinline__ unsigned phi(unsigned a, unsigned b) { return __builtin_amdgcn_perm(b, a, 0x07060302u); }
__device__ __forceinline__ float exp_(float x) { return __builtin_amdgcn_exp2f(x * 1.4426950408889634f); }

#define STAMP(i) do { } while (0)

template <int N> struct IC { static constexpr int value = N; constexpr operator int() const { return N; } };
template <typename Fn, int... I> __device__ __forceinline__ void sfor_impl(Fn&& f, std::integer_sequence<int, I...>) { (f(IC<I>{}), ...); }
template <int N, typename Fn> __device__ __forceinline__ void sfor(Fn&& f) { sfor_impl(f, std::make_integer_sequence<int, N>{}); }
#define ROT 1
#define EARLY 1
#define EPS 1
#define XOL 1
#define PT 0
#define PTP 0
#define VT 0
#define SPERM 2
#define QMAP 0
#define RSL 0
#define RMISS 3
#define CPF 0
#define HMAP 1
#define PSEQ "wwwKwwwKwwwKwwwKwwwKwwwKwwK"
#define WN 8
#define WN2 WN
#define XB 2
#define T_D2 32
#define T_D3 32
constexpr char pseq[] = PSEQ;
constexpr int PLEN = sizeof(pseq) - 1;
constexpr int pcount(int n, char ch) { int a = 0; for (int i = 0; i < n; ++i) a += pseq[i] == ch; return a; }
constexpr int PK = pcount(PLEN, 'K');
constexpr int PNL = 20 + 4 * PK;
static_assert(pcount(PLEN, 'w') == 20, "20 wq loads");
constexpr int pload(int n) { int k = 0; for (int i = 0; i < PLEN; ++i) { const int len = pseq[i] == 'w' ? 1 : 4; if (n < k + len) return pseq[i] == 'w' ? pcount(i, 'w') : -(1 + 4 * pcount(i, 'K') + (n - k)); k += len; } return 0; }
constexpr int wq_done(int n) { int a = 0; for (int i = 0; i < n && i < PNL; ++i) a += pload(i) >= 0; return a / 2; }
constexpr int icum(int t) {
  int v = PK + (XB >= 1 ? 1 : 0);
  if (XB >= 2 && t >= PK / 2) v = PK + 2;
  if (XB >= 3 && t >= PK - 1) v = PK + 3;
  const int d = t >= T_D3 ? 3 : (t >= T_D2 ? 2 : 1);
  if (t + 1 + d > v) v = t + 1 + d;
  return v > 32 ? 32 : v;
}
#define T_X0 (PK + XB - 1)
#define T_X1 29
#define GUW 8
constexpr int xstep(int i) { const int cw = i < 10 ? GUW * i : GUW * 10 + 5 * (i - 10); return T_X0 + cw * (T_X1 - T_X0) / (GUW * 10 + 100); }
constexpr int xcum(int t) { int a = 0; for (int i = 0; i < 30; ++i) a += xstep(i) < t; return a; }
constexpr int pcum(int t) { const int n = xcum(t - 2) - 14; return n < 0 ? 0 : n / 2; }
constexpr int T_FF = xstep(9) + 2;
constexpr int T_H = xstep(13) + 2 > T_FF + 3 ? xstep(13) + 2 : T_FF + 3;
constexpr int T_ML = 28;
static_assert(pcum(32) == 8 && T_H < 31, "schedule must finish inside the stream");
constexpr int vmcnt_imm(int n) { return (n & 15) | ((n >> 4) << 14) | 0x0F70; }

struct Smem {
  unsigned xn[8][320];
  float qpart[8][128];
  float fpart[8][64];
  unsigned qv[8][64];
  float sc[8][256];
  float opart[8][128];
  float ypart[8][640];
  float red[8][128];
  unsigned cnt;
};

__global__ void __launch_bounds__(512, 2)
fused_decode(const unsigned short* __restrict__ x, const unsigned short* __restrict__ kc, const unsigned short* __restrict__ vc,
             const unsigned short* __restrict__ wq, const unsigned short* __restrict__ wo, const unsigned short* __restrict__ wg,
             const unsigned short* __restrict__ wu, const unsigned short* __restrict__ wd, unsigned short* __restrict__ out,
             unsigned char* ws) {
  __shared__ __attribute__((aligned(16))) Smem sm;
  const int tid = threadIdx.x, lane = tid & 63;
  const int w = __builtin_amdgcn_readfirstlane(tid >> 6);
  const int bid = blockIdx.x, x8 = bid & 7, rr_ = bid >> 3, half = rr_ & 1, b = (rr_ >> 1) & 3, hhi = rr_ >> 3, h = HMAP ? x8 * 4 + hhi : hhi * 8 + x8;
  const int r = lane >> 4, c = lane & 15;
  STAMP(0);
  asm volatile("" :: "s"(x), "s"(kc), "s"(vc), "s"(wq), "s"(wo), "s"(wg), "s"(wu), "s"(wd), "s"(out), "s"(ws));

  const unsigned region = (unsigned)((b * H + h) * 2 + half);
  auto rX = __builtin_amdgcn_make_buffer_rsrc((void*)x, 0, 4 * D * 2, 0x00020000);
  auto rK = __builtin_amdgcn_make_buffer_rsrc((void*)(kc + (size_t)region * (2048 * 128)), 0, 512 * 1024, 0x00020000);
  auto rV = __builtin_amdgcn_make_buffer_rsrc((void*)(vc + (size_t)region * (2048 * 128)), 0, 512 * 1024, 0x00020000);
  auto rWq = __builtin_amdgcn_make_buffer_rsrc((void*)wq, 0, D * 4096 * 2, 0x00020000);
  auto rWo = __builtin_amdgcn_make_buffer_rsrc((void*)wo, 0, D * 4096 * 2, 0x00020000);
  auto rWs = __builtin_amdgcn_make_buffer_rsrc((void*)ws, 0, WS_BYTES, 0x00020000);
  const int cls = (hhi + (int)((unsigned long long)wq >> 8) + 2) & 3;
  const bool has_ffn = cls < 3 || (x8 & 3) != 3, full_ffn = cls < 3;
  const int fcol = cls < 3 ? x8 * 192 + cls * 64 + half * 32 : 1536 + (x8 - (x8 >> 2)) * 32 + half * 16;
  static_assert(PT == 0, "prefetch map");
  auto rWg = __builtin_amdgcn_make_buffer_rsrc((void*)wg, 0, has_ffn ? D * F * 2 : 0, 0x00020000);
  auto rWu = __builtin_amdgcn_make_buffer_rsrc((void*)wu, 0, has_ffn ? D * F * 2 : 0, 0x00020000);
  auto rWd = __builtin_amdgcn_make_buffer_rsrc((void*)wd, 0, has_ffn && (full_ffn || w < 4) ? F * D * 2 : 0, 0x00020000);
  const int rj = (h - b * 5) & 31;
  const bool is_red = (half == 0) && (rj < 5);
  const int gp = rj * 64 + lane;
  const int ocol = gp < 128 ? 8 * (gp >> 1) + 2 * (gp & 1) : (gp < 256 ? 8 * ((gp - 128) >> 1) + 4 + 2 * (gp & 1) : 512 + 2 * (gp - 256));

  u32x4 cpf;
  if constexpr (CPF) {
    unsigned long long pc; unsigned len;
    asm volatile("s_getpc_b64 %0\n.Lpf_begin:\n\ts_mov_b32 %1, .Lpf_end-.Lpf_begin" : "=s"(pc), "=s"(len));
    auto rC = __builtin_amdgcn_make_buffer_rsrc((void*)(pc & ~15ull), 0, w == 0 ? len : 0, 0x00020000);
    cpf = LD128(rC, lane * 16 + rr_ * 1024, 0, 0);
    SB();
  }
  const u32x4 xa = LD128(rX, lane * 16, b * 1280, 0);
  const unsigned xb = LD32(rX, 1024 + lane * 4, b * 1280, 0);
  const u32x2 ep = *(const __attribute__((address_space(4))) u32x2*)((unsigned long long)ws + WS_EPOCH + b * 256);
  if (tid == 0) sm.cnt = 0;
  SB();
  unsigned pt[5];
  auto touch = [&](auto Q) __attribute__((always_inline)) {
    constexpr int q = Q;
    const unsigned wi = rr_ * 8 + w, nl = x8 < 3 ? 4 : 3, f0 = (x8 < 3 ? 4 * x8 : 3 * x8 + 3) * 64, off = 0x80000000u;
    if constexpr (q & 1) { pt[0] = LD32(rWo, lane < 40 ? lane * 64 : off, (x8 * 256 + wi) * 2560, WO_AUX); SB(); }
    if constexpr (q & 2) {
      auto rA = __builtin_amdgcn_make_buffer_rsrc((void*)wd, 0, F * D * 2, 0x00020000);
      pt[1] = LD32(rA, lane < 5 * nl ? lane * 64 : off, f0 * 1280 + wi * nl * 320, FF_AUX); SB();
    }
    if constexpr (q & 4) {
      auto rA = __builtin_amdgcn_make_buffer_rsrc((void*)wg, 0, F * D * 2, 0x00020000);
      auto rB = __builtin_amdgcn_make_buffer_rsrc((void*)wu, 0, F * D * 2, 0x00020000);
      const unsigned ch = wi * 20 + lane, vo = lane < 20 && (ch & 7) < 2 * nl ? (ch >> 3) * 3456 + (ch & 7) * 64 : off;
      pt[2] = LD32(rA, vo, f0 * 2, FF_AUX); SB();
      pt[3] = LD32(rB, vo, f0 * 2, FF_AUX); SB();
    }
    if constexpr (q & 8) {
      const unsigned ch = wi * 40 + lane;
      pt[4] = LD32(rWq, lane < 40 ? (ch >> 4) * 8192 + (ch & 15) * 64 : off, x8 * 1024, WQ_AUX); SB();
    }
  };
  auto untouch = [&](auto Q) __attribute__((always_inline)) {
    constexpr int q = Q;
    if constexpr (q & 1) asm volatile("" :: "v"(pt[0]));
    if constexpr (q & 2) asm volatile("" :: "v"(pt[1]));
    if constexpr (q & 4) asm volatile("" :: "v"(pt[2]), "v"(pt[3]));
    if constexpr (q & 8) asm volatile("" :: "v"(pt[4]));
  };
  static_assert(HMAP == 1 || PT == 0, "prefetch map");
  unsigned vt0, vt1;
  if constexpr (VT > 0) {
    vt0 = LD32(rK, lane < VT ? lane * (65536 / VT) : 0x80000000u, w * 65536, K_AUX); SB();
    vt1 = LD32(rV, lane < VT ? lane * (65536 / VT) : 0x80000000u, w * 65536, V_AUX); SB();
  }
  touch(IC<PT & 8>{});
  if constexpr (PTP == 0) touch(IC<PT & 7>{});
  u32x4 wqv[20];
  u32x4 kv[33][4];
  const unsigned kvo = lane * 16;
  const int rot = ROT == 0 ? 0 : (ROT == 1 ? rr_ : (ROT == 2 ? 2 * x8 : rr_ + 2 * x8 + w));
  const unsigned wq_vo = r * ((QMAP == 0 ? 20 : (QMAP == 3 ? 160 : 2)) * 8192) + c * 16, wq_so = w * ((QMAP == 2 ? 8 : (QMAP == 3 ? 20 : 80)) * 8192) + h * 256;
  auto ld_kv1 = [&](auto T, auto J) __attribute__((always_inline)) {
    constexpr int t = T, j = J;
    if constexpr (t < 16) kv[t][j] = LD128(rK, kvo + j * 1024, (((t + rot) & 15) * 8 + w) * 4096, K_AUX);
    else if constexpr (t < 32) kv[t][j] = LD128(rV, kvo + j * 1024, (((t + rot) & 15) * 8 + w) * 4096, V_AUX);
    SB();
  };
  auto ld_kv = [&](auto T) __attribute__((always_inline)) { sfor<4>([&](auto J) __attribute__((always_inline)) { ld_kv1(T, J); }); };
  auto ld_pro = [&](auto N) __attribute__((always_inline)) {
    constexpr int p = pload(N);
    if constexpr (p >= 0) { wqv[p] = LD128(rWq, wq_vo, wq_so + (QMAP == 0 || QMAP == 3 ? p : (QMAP == 1 ? 8 : 64) * (p / 2) + (p & 1)) * 8192, WQ_AUX); SB(); }
    else ld_kv1(IC<(-p - 1) / 4>{}, IC<(-p - 1) % 4>{});
  };
  sfor<WN>([&](auto N) __attribute__((always_inline)) { ld_pro(N); });
  {
    float v[10] = {blo(xa[0]), bhi(xa[0]), blo(xa[1]), bhi(xa[1]), blo(xa[2]), bhi(xa[2]), blo(xa[3]), bhi(xa[3]), blo(xb), bhi(xb)};
    float s = 0.f;
#pragma unroll
    for (int i = 0; i < 10; ++i) s += v[i];
    const float mean = wave_sum(s) * (1.f / D);
    float s2 = 0.f;
#pragma unroll
    for (int i = 0; i < 10; ++i) { const float d = v[i] - mean; s2 += d * d; }
    const float var = wave_sum(s2) * (1.f / D);
    const float mb = rbf(mean), den = rbf(__builtin_sqrtf(rbf(rbf(var) + 1e-5f))), rd = 1.f / den;
#pragma unroll
    for (int i = 0; i < 10; ++i) v[i] = rbf(v[i] - mb) * rd;
    u32x4 o4 = {pk(v[0], v[1]), pk(v[2], v[3]), pk(v[4], v[5]), pk(v[6], v[7])};
    *(u32x4*)&sm.xn[w][lane * 4] = o4;
    sm.xn[w][256 + lane] = pk(v[8], v[9]);
  }
  CB();
  {
    unsigned xp[10];
    if constexpr (QMAP == 0 || QMAP == 3) {
      const unsigned* xs = &sm.xn[w][QMAP == 0 ? 40 * w + 10 * r : 80 * r + 10 * w];
#pragma unroll
      for (int i = 0; i < 5; ++i) { const u32x2 t = *(const u32x2*)(xs + 2 * i); xp[2 * i] = t[0]; xp[2 * i + 1] = t[1]; }
    } else {
      const unsigned* xs = &sm.xn[w][(QMAP == 1 ? 40 : 4) * w + r];
#pragma unroll
      for (int i = 0; i < 10; ++i) xp[i] = xs[(QMAP == 1 ? 4 : 32) * i];
    }
    float a[8] = {0.f, 0.f, 0.f, 0.f, 0.f, 0.f, 0.f, 0.f};
    SB();
    auto qpair = [&](auto I) __attribute__((always_inline)) {
      constexpr int i = I;
#pragma unroll
      for (int k = 0; k < 4; ++k) {
        a[2 * k] = dot2(plo(wqv[2 * i][k], wqv[2 * i + 1][k]), xp[i], a[2 * k]);
        a[2 * k + 1] = dot2(phi(wqv[2 * i][k], wqv[2 * i + 1][k]), xp[i], a[2 * k + 1]);
      }
      SB();
    };
    const int late = __builtin_amdgcn_readfirstlane(((hhi + (int)((unsigned long long)wq >> 8)) & 3) == 1);
    sfor<PNL - WN>([&](auto I) __attribute__((always_inline)) {
      constexpr int n = I + WN;
      if constexpr (WN2 == WN) __builtin_amdgcn_s_waitcnt(vmcnt_imm(WN - 1));
      else {
        asm volatile("s_cmp_lg_u32 %0, 0\n\ts_cbranch_scc1 1f\n\ts_waitcnt vmcnt(%1)\n1:" :: "s"(late), "n"(WN - 1) : "scc");
        __builtin_amdgcn_s_waitcnt(vmcnt_imm(WN2 - 1));
      }
      SB();
      ld_pro(IC<n>{});
      if constexpr (n >= WN2) sfor<wq_done(n - WN2 + 1) - wq_done(n - WN2)>([&](auto J) __attribute__((always_inline)) { qpair(IC<wq_done(n - WN2) + J>{}); });
    });
    sfor<10 - wq_done(PNL - WN2)>([&](auto J) __attribute__((always_inline)) { qpair(IC<wq_done(PNL - WN2) + J>{}); });
    float e[4];
#pragma unroll
    for (int k = 0; k < 4; ++k) e[k] = swap32_add(a[k], a[k + 4]);
    f32x2 g = {swap16_add(e[0], e[2]), swap16_add(e[1], e[3])};
    *(f32x2*)&sm.qpart[w][8 * c + 2 * r] = g;
  }
  if constexpr (CPF) asm volatile("" :: "v"(cpf));
  if constexpr (VT > 0) asm volatile("" :: "v"(vt0), "v"(vt1));
  untouch(IC<PTP == 0 ? PT : (PT & 8)>{});
  STAMP(1);
  BARRIER();
  if constexpr (PTP == 1) touch(IC<PT & 7>{});
  sfor<icum(-1) - PK>([&](auto I) __attribute__((always_inline)) { ld_kv(IC<PK + I>{}); });
  u32x4 qp;
  {
    f32x2 s = *(const f32x2*)&sm.qpart[0][2 * lane];
#pragma unroll
    for (int i = 1; i < 8; ++i) s += *(const f32x2*)&sm.qpart[i][2 * lane];
    sm.qv[w][lane] = pk(s[0], s[1]);
    CB();
    qp = *(const u32x4*)&sm.qv[w][4 * c];
  }
  STAMP(2);

  const bool b3 = lane & 8, b2 = lane & 4;
  float* scw = &sm.sc[w][r * 4 + ((lane >> 3) & 1) + ((lane >> 1) & 2)];
  const unsigned* pr = (const unsigned*)&sm.sc[w][r * 4];
  const unsigned gu_vo = full_ffn || !(lane & 2) ? (lane >> 2) * (5 * 3456) + (lane & 3) * 16 : 0x80000000u, gu_so = w * (80 * 3456) + fcol * 2;
  u32x4 gv[5], uv[5];
  u32x4 ra[20]; unsigned rb[20];
  unsigned wp[8][10];
  float y[10];
  float acc[8] = {0.f, 0.f, 0.f, 0.f, 0.f, 0.f, 0.f, 0.f};
  float mw = 0.f, fw = 0.f;
  u32x4 ml;
  unsigned tagA = ep[0] + 1u, tagB = ep[1] + 1u;
  asm volatile("" : "+s"(tagA), "+s"(tagB));
  auto ld_row = [&](auto R) __attribute__((always_inline)) {
    constexpr int i = R;
    if constexpr (i < 4) { const unsigned so = (fcol + 4 * w + i) * 1280; ra[i] = LD128(rWd, lane * 16, so, FF_AUX); rb[i] = LD32(rWd, 1024 + lane * 4, so, FF_AUX); }
    else { const unsigned so = (h * 128 + 16 * w + (i - 4)) * 1280; ra[i] = LD128(rWo, lane * 16, so, WO_AUX); rb[i] = LD32(rWo, 1024 + lane * 4, so, WO_AUX); }
    SB();
  };
  sfor<32>([&](auto T) __attribute__((always_inline)) {
    constexpr int t = T;
    constexpr int nx = icum(t) > icum(t - 1) ? icum(t - 1) : 32;
    if constexpr (t == 16) {
      STAMP(4);
      if constexpr (PTP == 1) untouch(IC<PT & 7>{});
      CB();
      const f32x4 s4 = *(const f32x4*)&sm.sc[w][lane * 4];
      mw = wave_max(fmaxf(fmaxf(s4[0], s4[1]), fmaxf(s4[2], s4[3])));
      const float p0 = exp_(s4[0] - mw), p1 = exp_(s4[1] - mw), p2 = exp_(s4[2] - mw), p3 = exp_(s4[3] - mw);
      const unsigned q01 = pk(p0, p1), q23 = pk(p2, p3);
      u32x4 pw = {q01 & 0xffffu, q01 >> 16, q23 & 0xffffu, q23 >> 16};
      *(u32x4*)&sm.sc[w][lane * 4] = pw;
      const float lw = wave_sum((p0 + p1) + (p2 + p3));
      u32x4 g = {f2u(mw), tagA, f2u(lw), tagB};
      ST128(g, rWs, lane == 0 ? 0u : 0x80000000u, WS_ML + (b * H + h) * 256 + (half * 8 + w) * 16);
      CB();
      SB();
    }
    if constexpr (t == 31) {
      if (__builtin_expect(!__all(ml[1] == tagA && ml[3] == tagB), 0)) {
        unsigned spins = 0;
        do {
          __builtin_amdgcn_s_sleep(2);
          CB();
          ml = LD128(rWs, c * 16, WS_ML + (b * H + h) * 256, SC1);
        } while (!__all(ml[1] == tagA && ml[3] == tagB) && ++spins < SPIN_CAP);
      }
      const float mi = u2f(ml[0]), M = row_max(mi);
      const float L = row_sum(u2f(ml[2]) * exp_(mi - M));
      fw = exp_(mw - M) / L;
      asm volatile("" : "+v"(fw));
      SB();
    }
    if constexpr (t < 16) {
      float a[4];
      sfor<4>([&](auto J) __attribute__((always_inline)) {
        constexpr int j = J;
        if constexpr (EARLY) { asm volatile("" :: "v"(kv[t][j])); SB(); ld_kv1(IC<nx>{}, J); }
        float u = dot2(kv[t][j][0], qp[0], 0.f); u = dot2(kv[t][j][1], qp[1], u); u = dot2(kv[t][j][2], qp[2], u); a[j] = dot2(kv[t][j][3], qp[3], u);
        SB();
        if constexpr (!EARLY) ld_kv1(IC<nx>{}, J);
      });
      const float u0 = (b3 ? a[1] : a[0]) + dpp<ROR8>(b3 ? a[0] : a[1]);
      const float u1 = (b3 ? a[3] : a[2]) + dpp<ROR8>(b3 ? a[2] : a[3]);
      float v = (b2 ? u1 : u0) + dpp<HMIR>(b2 ? u0 : u1);
      v += dpp<XOR1>(v); v += dpp<XOR2>(v);
      scw[t * 16] = rbf(rbf(v) * 0.08838834764831845f);
    } else {
      const u32x4 pv = *(const u32x4*)(pr + (t - 16) * 16);
      sfor<4>([&](auto J) __attribute__((always_inline)) {
        constexpr int j = J;
        if constexpr (EARLY) { asm volatile("" :: "v"(kv[t][j])); SB(); ld_kv1(IC<nx>{}, J); }
        const unsigned pl = pv[j], ph = pl << 16;
#pragma unroll
        for (int k = 0; k < 4; ++k) { acc[2 * k] = dot2(kv[t][j][k], pl, acc[2 * k]); acc[2 * k + 1] = dot2(kv[t][j][k], ph, acc[2 * k + 1]); }
        SB();
        if constexpr (!EARLY) ld_kv1(IC<nx>{}, J);
      });
#pragma unroll
      for (int k = 0; k < 8; ++k) asm volatile("" : "+v"(acc[k]));
    }
    SB();
    if constexpr (t == PK - 1) STAMP(3);
    if constexpr (icum(t) > icum(t - 1) + 1) sfor<icum(t) - icum(t - 1) - 1>([&](auto I) __attribute__((always_inline)) { ld_kv(IC<icum(t - 1) + 1 + I>{}); });
    sfor<xcum(t + 1) - xcum(t)>([&](auto I) __attribute__((always_inline)) {
      constexpr int i = xcum(t) + I;
      if constexpr (i >= 10) ld_row(IC<i - 10>{});
      else if constexpr (i & 1) { uv[i / 2] = LD128(rWu, gu_vo, gu_so + (i / 2) * 3456, FF_AUX); SB(); }
      else { gv[i / 2] = LD128(rWg, gu_vo, gu_so + (i / 2) * 3456, FF_AUX); SB(); }
    });
    if constexpr (t == T_ML) { ml = LD128(rWs, c * 16, WS_ML + (b * H + h) * 256, SC1); SB(); }
    if constexpr (t == T_FF) {
      const unsigned short* xs = (const unsigned short*)&sm.xn[w][0] + 80 * w + 5 * (lane >> 2);
      float a[16];
#pragma unroll
      for (int k = 0; k < 16; ++k) a[k] = 0.f;
#pragma unroll
      for (int i = 0; i < 5; ++i) {
        const unsigned xl = xs[i], xh = xl << 16;
#pragma unroll
        for (int k = 0; k < 4; ++k) {
          a[2 * k] = dot2(gv[i][k], xl, a[2 * k]); a[2 * k + 1] = dot2(gv[i][k], xh, a[2 * k + 1]);
          a[8 + 2 * k] = dot2(uv[i][k], xl, a[8 + 2 * k]); a[9 + 2 * k] = dot2(uv[i][k], xh, a[9 + 2 * k]);
        }
      }
      float e[8];
#pragma unroll
      for (int k = 0; k < 8; ++k) e[k] = swap32_add(a[k], a[k + 8]);
      f32x4 f;
#pragma unroll
      for (int k = 0; k < 4; ++k) { float u = swap16_add(e[k], e[k + 4]); u += dpp<ROR4>(u); u += dpp<ROR8>(u); f[k] = u; }
      *(f32x4*)&sm.fpart[w][(lane >> 5) * 32 + (lane & 3) * 8 + ((lane >> 4) & 1) * 4] = f;
      CB();
      if (lane == 0) __hip_atomic_fetch_add(&sm.cnt, 1u, __ATOMIC_RELAXED, __HIP_MEMORY_SCOPE_WORKGROUP);
      CB();
      SB();
    }
    if constexpr (t == T_H) {
      unsigned spins = 0;
      while (__builtin_amdgcn_readfirstlane(__hip_atomic_load(&sm.cnt, __ATOMIC_RELAXED, __HIP_MEMORY_SCOPE_WORKGROUP)) != 8u && ++spins < SPIN_CAP)
        __builtin_amdgcn_s_sleep(1);
      CB();
      const float fsum = sm.fpart[lane >> 3][((lane >> 2) & 1) * 32 + 4 * w + (lane & 3)];
      float v = fsum + dpp<ROR8>(fsum);
      v = rbf(xrow_sum(v));
      const float gs = rbf(v / (1.f + exp_(-v)));
      const float hh = gs * dpp<SHL4>(v);
      const unsigned hp = pk(hh, dpp<XOR1>(hh));
      const unsigned h01 = __builtin_amdgcn_readlane(hp, 0), h23 = __builtin_amdgcn_readlane(hp, 2);
#pragma unroll
      for (int k = 0; k < 4; ++k) {
        y[2 * k] = dot2(plo(ra[2][k], ra[3][k]), h23, dot2(plo(ra[0][k], ra[1][k]), h01, 0.f));
        y[2 * k + 1] = dot2(phi(ra[2][k], ra[3][k]), h23, dot2(phi(ra[0][k], ra[1][k]), h01, 0.f));
      }
      y[8] = dot2(plo(rb[2], rb[3]), h23, dot2(plo(rb[0], rb[1]), h01, 0.f));
      y[9] = dot2(phi(rb[2], rb[3]), h23, dot2(phi(rb[0], rb[1]), h01, 0.f));
#pragma unroll
      for (int k = 0; k < 10; ++k) asm volatile("" : "+v"(y[k]));
      SB();
    }
    sfor<pcum(t + 1) - pcum(t)>([&](auto I) __attribute__((always_inline)) {
      constexpr int i = pcum(t) + I;
#pragma unroll
      for (int k = 0; k < 4; ++k) { wp[i][2 * k] = plo(ra[4 + 2 * i][k], ra[5 + 2 * i][k]); wp[i][2 * k + 1] = phi(ra[4 + 2 * i][k], ra[5 + 2 * i][k]); }
      wp[i][8] = plo(rb[4 + 2 * i], rb[5 + 2 * i]); wp[i][9] = phi(rb[4 + 2 * i], rb[5 + 2 * i]);
#pragma unroll
      for (int k = 0; k < 10; ++k) asm volatile("" : "+v"(wp[i][k]));
      SB();
    });
  });
  STAMP(5);
  {
    float e[4];
#pragma unroll
    for (int k = 0; k < 4; ++k) e[k] = swap32_add(acc[k], acc[k + 4]);
    f32x2 g = {swap16_add(e[0], e[2]) * fw, swap16_add(e[1], e[3]) * fw};
    *(f32x2*)&sm.opart[w][8 * c + 2 * r] = g;
  }
  BARRIER();
  {
    float v = sm.opart[2 * r][16 * w + c] + sm.opart[2 * r + 1][16 * w + c];
    v = xrow_sum(v);
    const unsigned op = pk(v, dpp<XOR1>(v));
#pragma unroll
    for (int i = 0; i < 8; ++i) {
      const unsigned oi = __builtin_amdgcn_readlane(op, 2 * i);
#pragma unroll
      for (int k = 0; k < 10; ++k) y[k] = dot2(wp[i][k], oi, y[k]);
    }
    f32x4 y0 = {y[0], y[1], y[2], y[3]}, y1 = {y[4], y[5], y[6], y[7]};
    f32x2 y2 = {y[8], y[9]};
    *(f32x4*)&sm.ypart[w][lane * 4] = y0;
    *(f32x4*)&sm.ypart[w][256 + lane * 4] = y1;
    *(f32x2*)&sm.ypart[w][512 + lane * 2] = y2;
  }
  BARRIER();
  if (w < 5) {
    f32x2 s = *(const f32x2*)&sm.ypart[0][2 * tid];
#pragma unroll
    for (int i = 1; i < 8; ++i) s += *(const f32x2*)&sm.ypart[i][2 * tid];
    u32x4 g = {f2u(s[0]), tagA, f2u(s[1]), tagB};
    ST128(g, rWs, tid * 16, WS_Y + (b * 64 + h * 2 + half) * 5120);
  }
  STAMP(6);
  if (!is_red) return;
  const unsigned xo = LD32(rX, ocol * 2, b * 1280, 0);
  {
    u32x4 g[8];
    const unsigned so = WS_Y + (b * 64 + w * 8) * 5120 + rj * 1024;
#pragma unroll
    for (int i = 0; i < 8; ++i) g[i] = LD128(rWs, lane * 16, so + i * 5120, SC1);
    unsigned spins = 0;
    for (;;) {
      unsigned miss = 0;
      CB();
#pragma unroll
      for (int i = 0; i < 8; ++i) miss |= __all(g[i][1] == tagA && g[i][3] == tagB) ? 0u : 1u << i;
      if (miss == 0 || ++spins > SPIN_CAP) break;
      if (RSL && __builtin_popcount(miss) >= RMISS) __builtin_amdgcn_s_sleep(RSL);
      CB();
#pragma unroll
      for (int i = 0; i < 8; ++i) if (miss >> i & 1) g[i] = LD128(rWs, lane * 16, so + i * 5120, SC1);
    }
    f32x2 s = {0.f, 0.f};
#pragma unroll
    for (int i = 0; i < 8; ++i) { s[0] += u2f(g[i][0]); s[1] += u2f(g[i][2]); }
    *(f32x2*)&sm.red[w][2 * lane] = s;
  }
  BARRIER();
  if (w == 0) {
    f32x2 s = *(const f32x2*)&sm.red[0][2 * lane];
#pragma unroll
    for (int i = 1; i < 8; ++i) s += *(const f32x2*)&sm.red[i][2 * lane];
    *(unsigned*)(out + b * D + ocol) = pk(blo(xo) + s[0], bhi(xo) + s[1]);
    if (rj == 0 && lane == 0) {
      u32x2 e = {tagA, tagB};
      __builtin_amdgcn_raw_buffer_store_b64(e, rWs, 0, WS_EPOCH + b * 256, SC1);
    }
  }
  STAMP(7);
  if constexpr (CPF) asm volatile(".Lpf_end:");
}

extern "C" void kernel_launch(void* const* d_in, const int* in_sizes, int n_in, void* d_out, int out_size, void* d_ws, size_t ws_size,
                              hipStream_t stream) {
  fused_decode<<<256, 512, 0, stream>>>((const unsigned short*)d_in[0], (const unsigned short*)d_in[1], (const unsigned short*)d_in[2],
                                        (const unsigned short*)d_in[3], (const unsigned short*)d_in[4], (const unsigned short*)d_in[6],
                                        (const unsigned short*)d_in[7], (const unsigned short*)d_in[8], (unsigned short*)d_out,
                                        (unsigned char*)d_ws);
}
```

```cpp
#include <hip/hip_runtime.h>
#include <hip/hip_bf16.h>
#include <stdint.h>
#include <utility>

typedef unsigned u32x4 __attribute__((ext_vector_type(4)));
typedef unsigned u32x2 __attribute__((ext_vector_type(2)));
typedef float f32x4 __attribute__((ext_vector_type(4)));
typedef float f32x2 __attribute__((ext_vector_type(2)));
typedef __bf16 bf16x2 __attribute__((ext_vector_type(2)));

#define W_AUX 0
#define WQ_AUX W_AUX
#define WO_AUX W_AUX
#define FF_AUX W_AUX
#define K_AUX 2
#define V_AUX 2
constexpr int SC1 = 16;
constexpr int D = 640, H = 32, F = 1728;
constexpr unsigned WS_EPOCH = 0, WS_ML = 4096, WS_Y = 65536, WS_BYTES = WS_Y + 4 * 64 * 5120;
constexpr unsigned SPIN_CAP = 1u << 18;

#define LD128(r, vo, so, aux) __builtin_amdgcn_raw_buffer_load_b128(r, vo, so, aux)
#define LD64(r, vo, so, aux) __builtin_amdgcn_raw_buffer_load_b64(r, vo, so, aux)
#define LD32(r, vo, so, aux) __builtin_amdgcn_raw_buffer_load_b32(r, vo, so, aux)
#define SB() __builtin_amdgcn_sched_barrier(0)
#define ST128(v, r, vo, so) do { __builtin_amdgcn_raw_buffer_store_b128(v, r, vo, so, SC1); SB(); asm volatile("s_nop 1"); SB(); } while (0)
#define CB() asm volatile("" ::: "memory")
#define BARRIER() asm volatile("s_waitcnt lgkmcnt(0)\n\ts_barrier" ::: "memory")

__device__ __forceinline__ float u2f(unsigned u) { return __builtin_bit_cast(float, u); }
__device__ __forceinline__ unsigned f2u(float f) { return __builtin_bit_cast(unsigned, f); }
__device__ __forceinline__ float blo(unsigned u) { return u2f(u << 16); }
__device__ __forceinline__ float bhi(unsigned u) { return u2f(u & 0xffff0000u); }
__device__ __forceinline__ unsigned pk(float lo, float hi) {
  f32x2 v = {lo, hi};
  return __builtin_bit_cast(unsigned, __builtin_convertvector(v, bf16x2));
}
__device__ __forceinline__ float rbf(float f) { return blo(pk(f, f)); }
__device__ __forceinline__ float dot2(unsigned a, unsigned b, float acc) {
  return __builtin_amdgcn_fdot2_f32_bf16(__builtin_bit_cast(bf16x2, a), __builtin_bit_cast(bf16x2, b), acc, false);
}
template <int CTRL> __device__ __forceinline__ float dpp(float x) {
  return u2f((unsigned)__builtin_amdgcn_mov_dpp((int)f2u(x), CTRL, 0xf, 0xf, true));
}
constexpr int XOR1 = 0xB1, XOR2 = 0x4E, HMIR = 0x141, ROR8 = 0x128, ROR4 = 0x124, SHL4 = 0x104;
__device__ __forceinline__ float swap32_add(float a, float b) {
  auto t = __builtin_amdgcn_permlane32_swap(f2u(a), f2u(b), false, false);
  return u2f(t[0]) + u2f(t[1]);
}
__device__ __forceinline__ float swap16_add(float a, float b) {
  auto t = __builtin_amdgcn_permlane16_swap(f2u(a), f2u(b), false, false);
  return u2f(t[0]) + u2f(t[1]);
}
__device__ __forceinline__ float xrow_sum(float x) { x = swap16_add(x, x); return swap32_add(x, x); }
__device__ __forceinline__ float xrow_max(float x) {
  auto s = __builtin_amdgcn_permlane16_swap(f2u(x), f2u(x), false, false);
  x = fmaxf(u2f(s[0]), u2f(s[1]));
  auto t = __builtin_amdgcn_permlane32_swap(f2u(x), f2u(x), false, false);
  return fmaxf(u2f(t[0]), u2f(t[1]));
}
__device__ __forceinline__ float row_sum(float v) { v += dpp<XOR1>(v); v += dpp<XOR2>(v); v += dpp<HMIR>(v); v += dpp<ROR8>(v); return v; }
__device__ __forceinline__ float row_max(float v) {
  v = fmaxf(v, dpp<XOR1>(v)); v = fmaxf(v, dpp<XOR2>(v)); v = fmaxf(v, dpp<HMIR>(v)); v = fmaxf(v, dpp<ROR8>(v)); return v;
}
__device__ __forceinline__ float wave_sum(float v) { return xrow_sum(row_sum(v)); }
__device__ __forceinline__ float wave_max(float v) { return xrow_max(row_max(v)); }
__device__ __forceinline__ unsigned plo(unsigned a, unsigned b) { return __builtin_amdgcn_perm(b, a, 0x05040100u); }
__device__ __forceinline__ unsigned phi(unsigned a, unsigned b) { return __builtin_amdgcn_perm(b, a, 0x07060302u); }
__device__ __forceinline__ float exp_(float x) { return __builtin_amdgcn_exp2f(x * 1.4426950408889634f); }

template <int N> struct IC { static constexpr int value = N; constexpr operator int() const { return N; } };
template <typename Fn, int... I> __device__ __forceinline__ void sfor_impl(Fn&& f, std::integer_sequence<int, I...>) { (f(IC<I>{}), ...); }
template <int N, typename Fn> __device__ __forceinline__ void sfor(Fn&& f) { sfor_impl(f, std::make_integer_sequence<int, N>{}); }
#define ROT 1
#define EARLY 1
#define PSEQ "wwwKwwwKwwwKwwwKwwwKwwwKwwK"
#define WN 8
#define XB 2
#define T_D2 32
#define T_D3 32
constexpr char pseq[] = PSEQ;
constexpr int PLEN = sizeof(pseq) - 1;
constexpr int pcount(int n, char ch) { int a = 0; for (int i = 0; i < n; ++i) a += pseq[i] == ch; return a; }
constexpr int PK = pcount(PLEN, 'K');
constexpr int PNL = 20 + 4 * PK;
static_assert(pcount(PLEN, 'w') == 20, "20 wq loads");
constexpr int pload(int n) { int k = 0; for (int i = 0; i < PLEN; ++i) { const int len = pseq[i] == 'w' ? 1 : 4; if (n < k + len) return pseq[i] == 'w' ? pcount(i, 'w') : -(1 + 4 * pcount(i, 'K') + (n - k)); k += len; } return 0; }
constexpr int wq_done(int n) { int a = 0; for (int i = 0; i < n && i < PNL; ++i) a += pload(i) >= 0; return a / 2; }
constexpr int icum(int t) {
  int v = PK + (XB >= 1 ? 1 : 0);
  if (XB >= 2 && t >= PK / 2) v = PK + 2;
  if (XB >= 3 && t >= PK - 1) v = PK + 3;
  const int d = t >= T_D3 ? 3 : (t >= T_D2 ? 2 : 1);
  if (t + 1 + d > v) v = t + 1 + d;
  return v > 32 ? 32 : v;
}
#define T_X0 (PK + XB - 1)
#define T_X1 29
#define GUW 8
constexpr int xstep(int i) { const int cw = i < 10 ? GUW * i : GUW * 10 + 5 * (i - 10); return T_X0 + cw * (T_X1 - T_X0) / (GUW * 10 + 100); }
constexpr int xcum(int t) { int a = 0; for (int i = 0; i < 30; ++i) a += xstep(i) < t; return a; }
constexpr int pcum(int t) { const int n = xcum(t - 2) - 14; return n < 0 ? 0 : n / 2; }
constexpr int T_FF = xstep(9) + 2;
constexpr int T_H = xstep(13) + 2 > T_FF + 3 ? xstep(13) + 2 : T_FF + 3;
constexpr int T_ML = 28;
static_assert(pcum(32) == 8 && T_H < 31, "schedule must finish inside the stream");
constexpr int vmcnt_imm(int n) { return (n & 15) | ((n >> 4) << 14) | 0x0F70; }

struct Smem {
  unsigned xn[8][320];
  float qpart[8][128];
  float fpart[8][64];
  unsigned qv[8][64];
  float sc[8][256];
  float opart[8][128];
  float ypart[8][640];
  float red[8][128];
  unsigned cnt;
};

__global__ void __launch_bounds__(512, 2)
fused_decode(const unsigned short* __restrict__ x, const unsigned short* __restrict__ kc, const unsigned short* __restrict__ vc,
             const unsigned short* __restrict__ wq, const unsigned short* __restrict__ wo, const unsigned short* __restrict__ wg,
             const unsigned short* __restrict__ wu, const unsigned short* __restrict__ wd, unsigned short* __restrict__ out,
             unsigned char* ws) {
  __shared__ __attribute__((aligned(16))) Smem sm;
  const int tid = threadIdx.x, lane = tid & 63;
  const int w = __builtin_amdgcn_readfirstlane(tid >> 6);
  const int bid = blockIdx.x, x8 = bid & 7, rr_ = bid >> 3, half = rr_ & 1, b = (rr_ >> 1) & 3, hhi = rr_ >> 3, h = x8 * 4 + hhi;
  const int r = lane >> 4, c = lane & 15;
  asm volatile("" :: "s"(x), "s"(kc), "s"(vc), "s"(wq), "s"(wo), "s"(wg), "s"(wu), "s"(wd), "s"(out), "s"(ws));

  const unsigned region = (unsigned)((b * H + h) * 2 + half);
  auto rX = __builtin_amdgcn_make_buffer_rsrc((void*)x, 0, 4 * D * 2, 0x00020000);
  auto rK = __builtin_amdgcn_make_buffer_rsrc((void*)(kc + (size_t)region * (2048 * 128)), 0, 512 * 1024, 0x00020000);
  auto rV = __builtin_amdgcn_make_buffer_rsrc((void*)(vc + (size_t)region * (2048 * 128)), 0, 512 * 1024, 0x00020000);
  auto rWq = __builtin_amdgcn_make_buffer_rsrc((void*)wq, 0, D * 4096 * 2, 0x00020000);
  auto rWo = __builtin_amdgcn_make_buffer_rsrc((void*)wo, 0, D * 4096 * 2, 0x00020000);
  auto rWs = __builtin_amdgcn_make_buffer_rsrc((void*)ws, 0, WS_BYTES, 0x00020000);
  const int cls = (int)(((unsigned)hhi + (unsigned)((unsigned long long)wq >> 8) + 2u) & 3u);
  const bool has_ffn = cls < 3 || (x8 & 3) != 3, full_ffn = cls < 3;
  const int fcol = cls < 3 ? x8 * 192 + cls * 64 + half * 32 : 1536 + (x8 - (x8 >> 2)) * 32 + half * 16;
  auto rWg = __builtin_amdgcn_make_buffer_rsrc((void*)wg, 0, has_ffn ? D * F * 2 : 0, 0x00020000);
  auto rWu = __builtin_amdgcn_make_buffer_rsrc((void*)wu, 0, has_ffn ? D * F * 2 : 0, 0x00020000);
  auto rWd = __builtin_amdgcn_make_buffer_rsrc((void*)wd, 0, has_ffn && (full_ffn || w < 4) ? F * D * 2 : 0, 0x00020000);
  const int rj = (h - b * 5) & 31;
  const bool is_red = (half == 0) && (rj < 5);
  const int gp = rj * 64 + lane;
  const int ocol = gp < 128 ? 8 * (gp >> 1) + 2 * (gp & 1) : (gp < 256 ? 8 * ((gp - 128) >> 1) + 4 + 2 * (gp & 1) : 512 + 2 * (gp - 256));

  const u32x4 xa = LD128(rX, lane * 16, b * 1280, 0);
  const unsigned xb = LD32(rX, 1024 + lane * 4, b * 1280, 0);
  const u32x2 ep = *(const __attribute__((address_space(4))) u32x2*)((unsigned long long)ws + WS_EPOCH + b * 256);
  if (tid == 0) sm.cnt = 0;
  SB();
  u32x4 wqv[20];
  u32x4 kv[33][4];
  const unsigned kvo = lane * 16;
  const int rot = ROT == 0 ? 0 : (ROT == 1 ? rr_ : (ROT == 2 ? 2 * x8 : rr_ + 2 * x8 + w));
  const unsigned wq_vo = r * (20 * 8192) + c * 16, wq_so = w * (80 * 8192) + h * 256;
  auto ld_kv1 = [&](auto T, auto J) __attribute__((always_inline)) {
    constexpr int t = T, j = J;
    if constexpr (t < 16) kv[t][j] = LD128(rK, kvo + j * 1024, (((t + rot) & 15) * 8 + w) * 4096, K_AUX);
    else if constexpr (t < 32) kv[t][j] = LD128(rV, kvo + j * 1024, (((t + rot) & 15) * 8 + w) * 4096, V_AUX);
    SB();
  };
  auto ld_kv = [&](auto T) __attribute__((always_inline)) { sfor<4>([&](auto J) __attribute__((always_inline)) { ld_kv1(T, J); }); };
  auto ld_pro = [&](auto N) __attribute__((always_inline)) {
    constexpr int p = pload(N);
    if constexpr (p >= 0) { wqv[p] = LD128(rWq, wq_vo, wq_so + p * 8192, WQ_AUX); SB(); }
    else ld_kv1(IC<(-p - 1) / 4>{}, IC<(-p - 1) % 4>{});
  };
  sfor<WN>([&](auto N) __attribute__((always_inline)) { ld_pro(N); });
  {
    float v[10] = {blo(xa[0]), bhi(xa[0]), blo(xa[1]), bhi(xa[1]), blo(xa[2]), bhi(xa[2]), blo(xa[3]), bhi(xa[3]), blo(xb), bhi(xb)};
    float s = 0.f;
#pragma unroll
    for (int i = 0; i < 10; ++i) s += v[i];
    const float mean = wave_sum(s) * (1.f / D);
    float s2 = 0.f;
#pragma unroll
    for (int i = 0; i < 10; ++i) { const float d = v[i] - mean; s2 += d * d; }
    const float var = wave_sum(s2) * (1.f / D);
    const float mb = rbf(mean), den = rbf(__builtin_sqrtf(rbf(rbf(var) + 1e-5f))), rd = 1.f / den;
#pragma unroll
    for (int i = 0; i < 10; ++i) v[i] = rbf(v[i] - mb) * rd;
    u32x4 o4 = {pk(v[0], v[1]), pk(v[2], v[3]), pk(v[4], v[5]), pk(v[6], v[7])};
    *(u32x4*)&sm.xn[w][lane * 4] = o4;
    sm.xn[w][256 + lane] = pk(v[8], v[9]);
  }
  CB();
  {
    unsigned xp[10];
    const unsigned* xs = &sm.xn[w][40 * w + 10 * r];
#pragma unroll
    for (int i = 0; i < 5; ++i) { const u32x2 t = *(const u32x2*)(xs + 2 * i); xp[2 * i] = t[0]; xp[2 * i + 1] = t[1]; }
    float a[8] = {0.f, 0.f, 0.f, 0.f, 0.f, 0.f, 0.f, 0.f};
    SB();
    auto qpair = [&](auto I) __attribute__((always_inline)) {
      constexpr int i = I;
#pragma unroll
      for (int k = 0; k < 4; ++k) {
        a[2 * k] = dot2(plo(wqv[2 * i][k], wqv[2 * i + 1][k]), xp[i], a[2 * k]);
        a[2 * k + 1] = dot2(phi(wqv[2 * i][k], wqv[2 * i + 1][k]), xp[i], a[2 * k + 1]);
      }
      SB();
    };
    sfor<PNL - WN>([&](auto I) __attribute__((always_inline)) {
      constexpr int n = I + WN;
      __builtin_amdgcn_s_waitcnt(vmcnt_imm(WN - 1));
      SB();
      ld_pro(IC<n>{});
      sfor<wq_done(n - WN + 1) - wq_done(n - WN)>([&](auto J) __attribute__((always_inline)) { qpair(IC<wq_done(n - WN) + J>{}); });
    });
    sfor<10 - wq_done(PNL - WN)>([&](auto J) __attribute__((always_inline)) { qpair(IC<wq_done(PNL - WN) + J>{}); });
    float e[4];
#pragma unroll
    for (int k = 0; k < 4; ++k) e[k] = swap32_add(a[k], a[k + 4]);
    f32x2 g = {swap16_add(e[0], e[2]), swap16_add(e[1], e[3])};
    *(f32x2*)&sm.qpart[w][8 * c + 2 * r] = g;
  }
  BARRIER();
  sfor<icum(-1) - PK>([&](auto I) __attribute__((always_inline)) { ld_kv(IC<PK + I>{}); });
  u32x4 qp;
  {
    f32x2 s = *(const f32x2*)&sm.qpart[0][2 * lane];
#pragma unroll
    for (int i = 1; i < 8; ++i) s += *(const f32x2*)&sm.qpart[i][2 * lane];
    sm.qv[w][lane] = pk(s[0], s[1]);
    CB();
    qp = *(const u32x4*)&sm.qv[w][4 * c];
  }

  const bool b3 = lane & 8, b2 = lane & 4;
  float* scw = &sm.sc[w][r * 4 + ((lane >> 3) & 1) + ((lane >> 1) & 2)];
  const unsigned* pr = (const unsigned*)&sm.sc[w][r * 4];
  const unsigned gu_vo = full_ffn || !(lane & 2) ? (lane >> 2) * (5 * 3456) + (lane & 3) * 16 : 0x80000000u, gu_so = w * (80 * 3456) + fcol * 2;
  u32x4 gv[5], uv[5];
  u32x4 ra[20]; unsigned rb[20];
  unsigned wp[8][10];
  float y[10];
  float acc[8] = {0.f, 0.f, 0.f, 0.f, 0.f, 0.f, 0.f, 0.f};
  float mw = 0.f, fw = 0.f;
  u32x4 ml;
  unsigned tagA = ep[0] + 1u, tagB = ep[1] + 1u;
  asm volatile("" : "+s"(tagA), "+s"(tagB));
  auto ld_row = [&](auto R) __attribute__((always_inline)) {
    constexpr int i = R;
    if constexpr (i < 4) { const unsigned so = (fcol + 4 * w + i) * 1280; ra[i] = LD128(rWd, lane * 16, so, FF_AUX); rb[i] = LD32(rWd, 1024 + lane * 4, so, FF_AUX); }
    else { const unsigned so = (h * 128 + 16 * w + (i - 4)) * 1280; ra[i] = LD128(rWo, lane * 16, so, WO_AUX); rb[i] = LD32(rWo, 1024 + lane * 4, so, WO_AUX); }
    SB();
  };
  sfor<32>([&](auto T) __attribute__((always_inline)) {
    constexpr int t = T;
    constexpr int nx = icum(t) > icum(t - 1) ? icum(t - 1) : 32;
    if constexpr (t == 16) {
      CB();
      const f32x4 s4 = *(const f32x4*)&sm.sc[w][lane * 4];
      mw = wave_max(fmaxf(fmaxf(s4[0], s4[1]), fmaxf(s4[2], s4[3])));
      const float p0 = exp_(s4[0] - mw), p1 = exp_(s4[1] - mw), p2 = exp_(s4[2] - mw), p3 = exp_(s4[3] - mw);
      const unsigned q01 = pk(p0, p1), q23 = pk(p2, p3);
      u32x4 pw = {q01 & 0xffffu, q01 >> 16, q23 & 0xffffu, q23 >> 16};
      *(u32x4*)&sm.sc[w][lane * 4] = pw;
      const float lw = wave_sum((p0 + p1) + (p2 + p3));
      u32x4 g = {f2u(mw), tagA, f2u(lw), tagB};
      ST128(g, rWs, lane == 0 ? 0u : 0x80000000u, WS_ML + (b * H + h) * 256 + (half * 8 + w) * 16);
      CB();
      SB();
    }
    if constexpr (t == 31) {
      if (__builtin_expect(!__all(ml[1] == tagA && ml[3] == tagB), 0)) {
        unsigned spins = 0;
        do {
          __builtin_amdgcn_s_sleep(2);
          CB();
          ml = LD128(rWs, c * 16, WS_ML + (b * H + h) * 256, SC1);
        } while (!__all(ml[1] == tagA && ml[3] == tagB) && ++spins < SPIN_CAP);
      }
      const float mi = u2f(ml[0]), M = row_max(mi);
      const float L = row_sum(u2f(ml[2]) * exp_(mi - M));
      fw = exp_(mw - M) / L;
      asm volatile("" : "+v"(fw));
      SB();
    }
    if constexpr (t < 16) {
      float a[4];
      sfor<4>([&](auto J) __attribute__((always_inline)) {
        constexpr int j = J;
        if constexpr (EARLY) { asm volatile("" :: "v"(kv[t][j])); SB(); ld_kv1(IC<nx>{}, J); }
        float u = dot2(kv[t][j][0], qp[0], 0.f); u = dot2(kv[t][j][1], qp[1], u); u = dot2(kv[t][j][2], qp[2], u); a[j] = dot2(kv[t][j][3], qp[3], u);
        SB();
        if constexpr (!EARLY) ld_kv1(IC<nx>{}, J);
      });
      const float u0 = (b3 ? a[1] : a[0]) + dpp<ROR8>(b3 ? a[0] : a[1]);
      const float u1 = (b3 ? a[3] : a[2]) + dpp<ROR8>(b3 ? a[2] : a[3]);
      float v = (b2 ? u1 : u0) + dpp<HMIR>(b2 ? u0 : u1);
      v += dpp<XOR1>(v); v += dpp<XOR2>(v);
      scw[t * 16] = rbf(rbf(v) * 0.08838834764831845f);
    } else {
      const u32x4 pv = *(const u32x4*)(pr + (t - 16) * 16);
      sfor<4>([&](auto J) __attribute__((always_inline)) {
        constexpr int j = J;
        if constexpr (EARLY) { asm volatile("" :: "v"(kv[t][j])); SB(); ld_kv1(IC<nx>{}, J); }
        const unsigned pl = pv[j], ph = pl << 16;
#pragma unroll
        for (int k = 0; k < 4; ++k) { acc[2 * k] = dot2(kv[t][j][k], pl, acc[2 * k]); acc[2 * k + 1] = dot2(kv[t][j][k], ph, acc[2 * k + 1]); }
        SB();
        if constexpr (!EARLY) ld_kv1(IC<nx>{}, J);
      });
#pragma unroll
      for (int k = 0; k < 8; ++k) asm volatile("" : "+v"(acc[k]));
    }
    SB();
    if constexpr (icum(t) > icum(t - 1) + 1) sfor<icum(t) - icum(t - 1) - 1>([&](auto I) __attribute__((always_inline)) { ld_kv(IC<icum(t - 1) + 1 + I>{}); });
    sfor<xcum(t + 1) - xcum(t)>([&](auto I) __attribute__((always_inline)) {
      constexpr int i = xcum(t) + I;
      if constexpr (i >= 10) ld_row(IC<i - 10>{});
      else if constexpr (i & 1) { uv[i / 2] = LD128(rWu, gu_vo, gu_so + (i / 2) * 3456, FF_AUX); SB(); }
      else { gv[i / 2] = LD128(rWg, gu_vo, gu_so + (i / 2) * 3456, FF_AUX); SB(); }
    });
    if constexpr (t == T_ML) { ml = LD128(rWs, c * 16, WS_ML + (b * H + h) * 256, SC1); SB(); }
    if constexpr (t == T_FF) {
      const unsigned short* xs = (const unsigned short*)&sm.xn[w][0] + 80 * w + 5 * (lane >> 2);
      float a[16];
#pragma unroll
      for (int k = 0; k < 16; ++k) a[k] = 0.f;
#pragma unroll
      for (int i = 0; i < 5; ++i) {
        const unsigned xl = xs[i], xh = xl << 16;
#pragma unroll
        for (int k = 0; k < 4; ++k) {
          a[2 * k] = dot2(gv[i][k], xl, a[2 * k]); a[2 * k + 1] = dot2(gv[i][k], xh, a[2 * k + 1]);
          a[8 + 2 * k] = dot2(uv[i][k], xl, a[8 + 2 * k]); a[9 + 2 * k] = dot2(uv[i][k], xh, a[9 + 2 * k]);
        }
      }
      float e[8];
#pragma unroll
      for (int k = 0; k < 8; ++k) e[k] = swap32_add(a[k], a[k + 8]);
      f32x4 f;
#pragma unroll
      for (int k = 0; k < 4; ++k) { float u = swap16_add(e[k], e[k + 4]); u += dpp<ROR4>(u); u += dpp<ROR8>(u); f[k] = u; }
      *(f32x4*)&sm.fpart[w][(lane >> 5) * 32 + (lane & 3) * 8 + ((lane >> 4) & 1) * 4] = f;
      CB();
      if (lane == 0) __hip_atomic_fetch_add(&sm.cnt, 1u, __ATOMIC_RELAXED, __HIP_MEMORY_SCOPE_WORKGROUP);
      CB();
      SB();
    }
    if constexpr (t == T_H) {
      unsigned spins = 0;
      while (__builtin_amdgcn_readfirstlane(__hip_atomic_load(&sm.cnt, __ATOMIC_RELAXED, __HIP_MEMORY_SCOPE_WORKGROUP)) != 8u && ++spins < SPIN_CAP)
        __builtin_amdgcn_s_sleep(1);
      CB();
      const float fsum = sm.fpart[lane >> 3][((lane >> 2) & 1) * 32 + 4 * w + (lane & 3)];
      float v = fsum + dpp<ROR8>(fsum);
      v = rbf(xrow_sum(v));
      const float gs = rbf(v / (1.f + exp_(-v)));
      const float hh = gs * dpp<SHL4>(v);
      const unsigned hp = pk(hh, dpp<XOR1>(hh));
      const unsigned h01 = __builtin_amdgcn_readlane(hp, 0), h23 = __builtin_amdgcn_readlane(hp, 2);
#pragma unroll
      for (int k = 0; k < 4; ++k) {
        y[2 * k] = dot2(plo(ra[2][k], ra[3][k]), h23, dot2(plo(ra[0][k], ra[1][k]), h01, 0.f));
        y[2 * k + 1] = dot2(phi(ra[2][k], ra[3][k]), h23, dot2(phi(ra[0][k], ra[1][k]), h01, 0.f));
      }
      y[8] = dot2(plo(rb[2], rb[3]), h23, dot2(plo(rb[0], rb[1]), h01, 0.f));
      y[9] = dot2(phi(rb[2], rb[3]), h23, dot2(phi(rb[0], rb[1]), h01, 0.f));
#pragma unroll
      for (int k = 0; k < 10; ++k) asm volatile("" : "+v"(y[k]));
      SB();
    }
    sfor<pcum(t + 1) - pcum(t)>([&](auto I) __attribute__((always_inline)) {
      constexpr int i = pcum(t) + I;
#pragma unroll
      for (int k = 0; k < 4; ++k) { wp[i][2 * k] = plo(ra[4 + 2 * i][k], ra[5 + 2 * i][k]); wp[i][2 * k + 1] = phi(ra[4 + 2 * i][k], ra[5 + 2 * i][k]); }
      wp[i][8] = plo(rb[4 + 2 * i], rb[5 + 2 * i]); wp[i][9] = phi(rb[4 + 2 * i], rb[5 + 2 * i]);
#pragma unroll
      for (int k = 0; k < 10; ++k) asm volatile("" : "+v"(wp[i][k]));
      SB();
    });
  });
  {
    float e[4];
#pragma unroll
    for (int k = 0; k < 4; ++k) e[k] = swap32_add(acc[k], acc[k + 4]);
    f32x2 g = {swap16_add(e[0], e[2]) * fw, swap16_add(e[1], e[3]) * fw};
    *(f32x2*)&sm.opart[w][8 * c + 2 * r] = g;
  }
  BARRIER();
  {
    float v = sm.opart[2 * r][16 * w + c] + sm.opart[2 * r + 1][16 * w + c];
    v = xrow_sum(v);
    const unsigned op = pk(v, dpp<XOR1>(v));
#pragma unroll
    for (int i = 0; i < 8; ++i) {
      const unsigned oi = __builtin_amdgcn_readlane(op, 2 * i);
#pragma unroll
      for (int k = 0; k < 10; ++k) y[k] = dot2(wp[i][k], oi, y[k]);
    }
    f32x4 y0 = {y[0], y[1], y[2], y[3]}, y1 = {y[4], y[5], y[6], y[7]};
    f32x2 y2 = {y[8], y[9]};
    *(f32x4*)&sm.ypart[w][lane * 4] = y0;
    *(f32x4*)&sm.ypart[w][256 + lane * 4] = y1;
    *(f32x2*)&sm.ypart[w][512 + lane * 2] = y2;
  }
  BARRIER();
  if (w < 5) {
    f32x2 s = *(const f32x2*)&sm.ypart[0][2 * tid];
#pragma unroll
    for (int i = 1; i < 8; ++i) s += *(const f32x2*)&sm.ypart[i][2 * tid];
    u32x4 g = {f2u(s[0]), tagA, f2u(s[1]), tagB};
    ST128(g, rWs, tid * 16, WS_Y + (b * 64 + h * 2 + half) * 5120);
  }
  if (!is_red) return;
  const unsigned xo = LD32(rX, ocol * 2, b * 1280, 0);
  {
    u32x4 g[8];
    const unsigned so = WS_Y + (b * 64 + w * 8) * 5120 + rj * 1024;
#pragma unroll
    for (int i = 0; i < 8; ++i) g[i] = LD128(rWs, lane * 16, so + i * 5120, SC1);
    unsigned spins = 0;
    for (;;) {
      unsigned miss = 0;
      CB();
#pragma unroll
      for (int i = 0; i < 8; ++i) miss |= __all(g[i][1] == tagA && g[i][3] == tagB) ? 0u : 1u << i;
      if (miss == 0 || ++spins > SPIN_CAP) break;
      CB();
#pragma unroll
      for (int i = 0; i < 8; ++i) if (miss >> i & 1) g[i] = LD128(rWs, lane * 16, so + i * 5120, SC1);
    }
    f32x2 s = {0.f, 0.f};
#pragma unroll
    for (int i = 0; i < 8; ++i) { s[0] += u2f(g[i][0]); s[1] += u2f(g[i][2]); }
    *(f32x2*)&sm.red[w][2 * lane] = s;
  }
  BARRIER();
  if (w == 0) {
    f32x2 s = *(const f32x2*)&sm.red[0][2 * lane];
#pragma unroll
    for (int i = 1; i < 8; ++i) s += *(const f32x2*)&sm.red[i][2 * lane];
    *(unsigned*)(out + b * D + ocol) = pk(blo(xo) + s[0], bhi(xo) + s[1]);
    if (rj == 0 && lane == 0) {
      u32x2 e = {tagA, tagB};
      __builtin_amdgcn_raw_buffer_store_b64(e, rWs, 0, WS_EPOCH + b * 256, SC1);
    }
  }
}

extern "C" void kernel_launch(void* const* d_in, const int* in_sizes, int n_in, void* d_out, int out_size, void* d_ws, size_t ws_size,
                              hipStream_t stream) {
  fused_decode<<<256, 512, 0, stream>>>((const unsigned short*)d_in[0], (const unsigned short*)d_in[1], (const unsigned short*)d_in[2],
                                        (const unsigned short*)d_in[3], (const unsigned short*)d_in[4], (const unsigned short*)d_in[6],
                                        (const unsigned short*)d_in[7], (const unsigned short*)d_in[8], (unsigned short*)d_out,
                                        (unsigned char*)d_ws);
}
```
